```python
import torch

def _softmax(x, axis=-1):
    return torch.softmax(x, dim=axis)
def _gelu(x):
    return torch.nn.functional.gelu(x, approximate="tanh")
def _silu(x):
    return torch.nn.functional.silu(x)
def _sigmoid(x):
    return torch.sigmoid(x)
def _layernorm(x, axis=-1, eps=1e-5):
    m = x.mean(dim=axis, keepdim=True)
    v = x.var(dim=axis, keepdim=True, unbiased=False)
    return (x - m) / torch.sqrt(v + eps)
def _scatter_add(tgt, idx, src, axis=0):
    return tgt.clone().index_add_(axis, idx, src)
def _segment_sum(src, seg, n):
    out = torch.zeros((n,) + src.shape[1:], dtype=src.dtype)
    return out.index_add_(0, seg, src)
def _pad(x, pw):
    flat = tuple(v for pair in reversed(pw) for v in pair)
    return torch.nn.functional.pad(x, flat)

kernel_name = 'recurrent_scan__compressed_kv_cla__dense_activation_relu__decode'

def setup_inputs():

    _w = (lambda a: (lambda t: t.to(torch.bfloat16) if t.is_floating_point() else t)(torch.as_tensor(a)))
    def _A_setup():
        B, H, Sq, Sk, D, M = 1, 32, 1, 16384, 128, 640
        Dc = 4 * D
        return {
            "x": _w((lambda *s: torch.randn(*s, dtype=torch.bfloat16) * 0.05)(B, Sq, M)),
            "c_kv": _w((lambda *s: torch.randn(*s, dtype=torch.bfloat16) * 0.8)(B, Sk, Dc)),
            "w_q": _w((lambda *s: torch.randn(*s, dtype=torch.bfloat16) * 1.6)(M, H, D) * (M ** -0.5)),
            "w_uk": _w((lambda *s: torch.randn(*s, dtype=torch.bfloat16) * 1.6)(Dc, H, D) * (Dc ** -0.5)),
            "w_uv": _w((lambda *s: torch.randn(*s, dtype=torch.bfloat16) * 0.8)(Dc, H, D) * (Dc ** -0.5)),
            "w_o": _w((lambda *s: torch.randn(*s, dtype=torch.bfloat16) * 0.8)(H, D, M) * ((H * D) ** -0.5)),
        }
    _A_in = _A_setup()

    def _B_setup():
        B, S, D, Dff = 1, 1, 640, 1728
        return {
            "x": _w((lambda *s: torch.randn(*s, dtype=torch.bfloat16) * 0.05)(B, S, D)),
            "w1": _w((lambda *s: torch.randn(*s, dtype=torch.bfloat16) * 0.2)(D, Dff)),
            "w2": _w((lambda *s: torch.randn(*s, dtype=torch.bfloat16) * 0.2)(Dff, D)),
        }
    _B_in = _B_setup()

    return {
        "A_x": _A_in["x"],
        "A_c_kv": _A_in["c_kv"],
        "A_w_q": _A_in["w_q"],
        "A_w_uk": _A_in["w_uk"],
        "A_w_uv": _A_in["w_uv"],
        "A_w_o": _A_in["w_o"],
        "B_x": _B_in["x"],
        "B_w1": _B_in["w1"],
        "B_w2": _B_in["w2"],
    }

def reference(A_x, A_c_kv, A_w_q, A_w_uk, A_w_uv, A_w_o, B_x, B_w1, B_w2):
    def _A_ref(x, c_kv, w_q, w_uk, w_uv, w_o):
        B, Sq, M = x.shape[0], x.shape[1], x.shape[2]
        Sk = c_kv.shape[1]
        H, D = w_q.shape[1], w_q.shape[2]
        q = torch.einsum("bqm,mhd->bhqd", x, w_q)
        k = torch.einsum("bkc,chd->bhkd", c_kv, w_uk)
        v = torch.einsum("bkc,chd->bhkd", c_kv, w_uv)
        scores = torch.einsum("bhqd,bhkd->bhqk", q, k) * (D ** -0.5)
        off = Sk - Sq
        qi = torch.arange(Sq)[:, None]
        kj = torch.arange(Sk)[None, :]
        cmask = kj <= (qi + off)
        scores = torch.where(cmask[None, None], scores, -1e9)
        p = _softmax(scores, axis=-1)
        ctx = torch.einsum("bhqk,bhkd->bhqd", p, v)
        return torch.einsum("bhqd,hdm->bqm", ctx, w_o)

    def _B_ref(x, w1, w2):
        h = torch.einsum("bsd,df->bsf", x, w1)
        zero = torch.zeros_like(h)
        h = torch.maximum(h, zero)
        return torch.einsum("bsf,fd->bsd", h, w2)

    x = A_x
    xn = _layernorm(x, axis=-1)
    s = _A_ref(xn, A_c_kv, A_w_q, A_w_uk, A_w_uv, A_w_o)
    g = _silu(_B_ref(xn, B_w1, B_w2))
    return x + s * g
```

```cpp
#include <hip/hip_runtime.h>
#include <hip/hip_bf16.h>
#include <stdint.h>

constexpr int M = 640, H = 32, D = 128, SK = 16384, DC = 512, DFF = 1728;

__device__ __forceinline__ float bf2f(unsigned short u) { return __uint_as_float(((unsigned)u) << 16); }

__device__ void layernorm_to_lds(const unsigned short* x, float* xn, float* red) {
  int t = threadIdx.x;
  float s = 0.f;
  for (int i = t; i < M; i += blockDim.x) { float v = bf2f(x[i]); xn[i] = v; s += v; }
  for (int o = 32; o; o >>= 1) s += __shfl_xor(s, o);
  if ((t & 63) == 0) red[t >> 6] = s;
  __syncthreads();
  float tot = 0.f;
  for (int i = 0; i < (int)(blockDim.x >> 6); ++i) tot += red[i];
  float mean = tot / M;
  __syncthreads();
  float v2 = 0.f;
  for (int i = t; i < M; i += blockDim.x) { float d = xn[i] - mean; v2 += d * d; }
  for (int o = 32; o; o >>= 1) v2 += __shfl_xor(v2, o);
  if ((t & 63) == 0) red[t >> 6] = v2;
  __syncthreads();
  float tv = 0.f;
  for (int i = 0; i < (int)(blockDim.x >> 6); ++i) tv += red[i];
  float rstd = rsqrtf(tv / M + 1e-5f);
  for (int i = t; i < M; i += blockDim.x) xn[i] = (xn[i] - mean) * rstd;
  __syncthreads();
}

__global__ void k1(const unsigned short* x, const unsigned short* wq, const unsigned short* w1, float* q, float* hid) {
  __shared__ float xn[M]; __shared__ float red[16];
  layernorm_to_lds(x, xn, red);
  int j = blockIdx.x * blockDim.x + threadIdx.x;
  if (j < H * D) { float a = 0; for (int m = 0; m < M; ++m) a += xn[m] * bf2f(wq[m * H * D + j]); q[j] = a; }
  else if (j < H * D + DFF) { int f = j - H * D; float a = 0; for (int m = 0; m < M; ++m) a += xn[m] * bf2f(w1[m * DFF + f]); hid[f] = fmaxf(a, 0.f); }
}
__global__ void k2(const float* q, const unsigned short* wuk, float* qabs) {
  int j = blockIdx.x * blockDim.x + threadIdx.x;
  int h = j >> 9, c = j & 511;
  float a = 0; for (int d = 0; d < D; ++d) a += q[h * D + d] * bf2f(wuk[(c * H + h) * D + d]);
  qabs[j] = a * 0.08838834764831845f;
}
__global__ void k3(const float* qabs, const unsigned short* ckv, float* sc) {
  int j = blockIdx.x * blockDim.x + threadIdx.x;
  int k = j >> 5, h = j & 31;
  float a = 0; for (int c = 0; c < DC; ++c) a += qabs[h * DC + c] * bf2f(ckv[k * DC + c]);
  sc[h * SK + k] = a;
}
__global__ void k4(float* sc) {
  __shared__ float red[16];
  float* s = sc + blockIdx.x * SK; int t = threadIdx.x;
  float m = -1e30f; for (int i = t; i < SK; i += blockDim.x) m = fmaxf(m, s[i]);
  for (int o = 32; o; o >>= 1) m = fmaxf(m, __shfl_xor(m, o));
  if ((t & 63) == 0) red[t >> 6] = m; __syncthreads();
  m = -1e30f; for (int i = 0; i < (int)(blockDim.x >> 6); ++i) m = fmaxf(m, red[i]); __syncthreads();
  float l = 0; for (int i = t; i < SK; i += blockDim.x) { float e = __expf(s[i] - m); s[i] = e; l += e; }
  for (int o = 32; o; o >>= 1) l += __shfl_xor(l, o);
  if ((t & 63) == 0) red[t >> 6] = l; __syncthreads();
  l = 0; for (int i = 0; i < (int)(blockDim.x >> 6); ++i) l += red[i];
  float inv = 1.f / l; for (int i = t; i < SK; i += blockDim.x) s[i] *= inv;
}
__global__ void k5(const float* p, const unsigned short* ckv, float* part) {
  int split = blockIdx.x >> 5, h = blockIdx.x & 31, c = threadIdx.x;
  float a = 0; for (int k = split * 256; k < split * 256 + 256; ++k) a += p[h * SK + k] * bf2f(ckv[k * DC + c]);
  part[(split * H + h) * DC + c] = a;
}
__global__ void k6(const float* part, const unsigned short* wuv, float* ctx) {
  __shared__ float cc[DC];
  int h = blockIdx.x, t = threadIdx.x;
  for (int c = t; c < DC; c += blockDim.x) { float a = 0; for (int s = 0; s < 64; ++s) a += part[(s * H + h) * DC + c]; cc[c] = a; }
  __syncthreads();
  if (t < D) { float a = 0; for (int c = 0; c < DC; ++c) a += cc[c] * bf2f(wuv[(c * H + h) * D + t]); ctx[h * D + t] = a; }
}
__global__ void k7(const unsigned short* x, const float* ctx, const unsigned short* wo, const float* hid, const unsigned short* w2, __hip_bfloat16* out) {
  __shared__ float red[2][4][64];
  int m = blockIdx.x * 64 + (threadIdx.x & 63); int w = threadIdx.x >> 6;
  float s = 0; for (int j = w; j < H * D; j += 4) s += ctx[j] * bf2f(wo[j * M + m]);
  float y = 0; for (int f = w; f < DFF; f += 4) y += hid[f] * bf2f(w2[f * M + m]);
  red[0][w][threadIdx.x & 63] = s; red[1][w][threadIdx.x & 63] = y; __syncthreads();
  if (w == 0) { int l = threadIdx.x; s = red[0][0][l] + red[0][1][l] + red[0][2][l] + red[0][3][l]; y = red[1][0][l] + red[1][1][l] + red[1][2][l] + red[1][3][l];
    float g = y / (1.f + __expf(-y)); out[m] = __float2bfloat16(bf2f(x[m]) + s * g); }
}

extern "C" void kernel_launch(void* const* d_in, const int* in_sizes, int n_in, void* d_out, int out_size, void* d_ws, size_t ws_size, hipStream_t stream) {
  const unsigned short* x = (const unsigned short*)d_in[0];
  const unsigned short* ckv = (const unsigned short*)d_in[1];
  const unsigned short* wq = (const unsigned short*)d_in[2];
  const unsigned short* wuk = (const unsigned short*)d_in[3];
  const unsigned short* wuv = (const unsigned short*)d_in[4];
  const unsigned short* wo = (const unsigned short*)d_in[5];
  const unsigned short* w1 = (const unsigned short*)d_in[7];
  const unsigned short* w2 = (const unsigned short*)d_in[8];
  float* ws = (float*)d_ws;
  float* q = ws; float* hid = ws + 4096; float* qabs = ws + 8192; float* ctx = ws + 8192 + 16384; float* sc = ws + 65536; float* part = sc + H * SK;
  k1<<<(H * D + DFF + 63) / 64, 64, 0, stream>>>(x, wq, w1, q, hid);
  k2<<<H * DC / 64, 64, 0, stream>>>(q, wuk, qabs);
  k3<<<SK * H / 256, 256, 0, stream>>>(qabs, ckv, sc);
  k4<<<H, 1024, 0, stream>>>(sc);
  k5<<<64 * H, 512, 0, stream>>>(sc, ckv, part);
  k6<<<H, 512, 0, stream>>>(part, wuv, ctx);
  k7<<<M / 64, 256, 0, stream>>>(x, ctx, wo, hid, w2, (__hip_bfloat16*)d_out);
}
```

```cpp
#include <hip/hip_runtime.h>
#include <hip/hip_bf16.h>
#include <stdint.h>

constexpr int M = 640, H = 32, D = 128, SK = 16384, DC = 512, DFF = 1728;

__device__ __forceinline__ float bf2f(unsigned short u) { return __uint_as_float(((unsigned)u) << 16); }

__device__ void layernorm_to_lds(const unsigned short* x, float* xn, float* red) {
  int t = threadIdx.x;
  float s = 0.f;
  for (int i = t; i < M; i += blockDim.x) { float v = bf2f(x[i]); xn[i] = v; s += v; }
  for (int o = 32; o; o >>= 1) s += __shfl_xor(s, o);
  if ((t & 63) == 0) red[t >> 6] = s;
  __syncthreads();
  float tot = 0.f;
  for (int i = 0; i < (int)(blockDim.x >> 6); ++i) tot += red[i];
  float mean = tot / M;
  __syncthreads();
  float v2 = 0.f;
  for (int i = t; i < M; i += blockDim.x) { float d = xn[i] - mean; v2 += d * d; }
  for (int o = 32; o; o >>= 1) v2 += __shfl_xor(v2, o);
  if ((t & 63) == 0) red[t >> 6] = v2;
  __syncthreads();
  float tv = 0.f;
  for (int i = 0; i < (int)(blockDim.x >> 6); ++i) tv += red[i];
  float rstd = rsqrtf(tv / M + 1e-5f);
  for (int i = t; i < M; i += blockDim.x) xn[i] = (xn[i] - mean) * rstd;
  __syncthreads();
}

__global__ void k1(const unsigned short* x, const unsigned short* wq, const unsigned short* w1, float* q, float* hid) {
  __shared__ float xn[M]; __shared__ float red[16];
  layernorm_to_lds(x, xn, red);
  int j = blockIdx.x * blockDim.x + threadIdx.x;
  if (j < H * D) { float a = 0; for (int m = 0; m < M; ++m) a += xn[m] * bf2f(wq[m * H * D + j]); q[j] = a; }
  else if (j < H * D + DFF) { int f = j - H * D; float a = 0; for (int m = 0; m < M; ++m) a += xn[m] * bf2f(w1[m * DFF + f]); hid[f] = fmaxf(a, 0.f); }
}
__global__ void k2(const float* q, const unsigned short* wuk, float* qabs) {
  int j = blockIdx.x * blockDim.x + threadIdx.x;
  int h = j >> 9, c = j & 511;
  float a = 0; for (int d = 0; d < D; ++d) a += q[h * D + d] * bf2f(wuk[(c * H + h) * D + d]);
  qabs[j] = a * 0.08838834764831845f;
}
__global__ void k3(const float* qabs, const unsigned short* ckv, float* sc) {
  int j = blockIdx.x * blockDim.x + threadIdx.x;
  int k = j >> 5, h = j & 31;
  float a = 0; for (int c = 0; c < DC; ++c) a += qabs[h * DC + c] * bf2f(ckv[k * DC + c]);
  sc[h * SK + k] = a;
}
__global__ void k4(float* sc) {
  __shared__ float red[16];
  float* s = sc + blockIdx.x * SK; int t = threadIdx.x;
  float m = -1e30f; for (int i = t; i < SK; i += blockDim.x) m = fmaxf(m, s[i]);
  for (int o = 32; o; o >>= 1) m = fmaxf(m, __shfl_xor(m, o));
  if ((t & 63) == 0) red[t >> 6] = m; __syncthreads();
  m = -1e30f; for (int i = 0; i < (int)(blockDim.x >> 6); ++i) m = fmaxf(m, red[i]); __syncthreads();
  float l = 0; for (int i = t; i < SK; i += blockDim.x) { float e = __expf(s[i] - m); s[i] = e; l += e; }
  for (int o = 32; o; o >>= 1) l += __shfl_xor(l, o);
  if ((t & 63) == 0) red[t >> 6] = l; __syncthreads();
  l = 0; for (int i = 0; i < (int)(blockDim.x >> 6); ++i) l += red[i];
  float inv = 1.f / l; for (int i = t; i < SK; i += blockDim.x) s[i] *= inv;
}
__global__ void k5(const float* p, const unsigned short* ckv, float* part) {
  int split = blockIdx.x >> 5, h = blockIdx.x & 31, c = threadIdx.x;
  float a = 0; for (int k = split * 256; k < split * 256 + 256; ++k) a += p[h * SK + k] * bf2f(ckv[k * DC + c]);
  part[(split * H + h) * DC + c] = a;
}
__global__ void k6(const float* part, const unsigned short* wuv, float* ctx) {
  __shared__ float cc[DC];
  int h = blockIdx.x, t = threadIdx.x;
  for (int c = t; c < DC; c += blockDim.x) { float a = 0; for (int s = 0; s < 64; ++s) a += part[(s * H + h) * DC + c]; cc[c] = a; }
  __syncthreads();
  if (t < D) { float a = 0; for (int c = 0; c < DC; ++c) a += cc[c] * bf2f(wuv[(c * H + h) * D + t]); ctx[h * D + t] = a; }
}
__global__ void k7(const unsigned short* x, const float* ctx, const unsigned short* wo, const float* hid, const unsigned short* w2, __hip_bfloat16* out) {
  __shared__ float red[2][4][64];
  int m = blockIdx.x * 64 + (threadIdx.x & 63); int w = threadIdx.x >> 6;
  float s = 0; for (int j = w; j < H * D; j += 4) s += ctx[j] * bf2f(wo[j * M + m]);
  float y = 0; for (int f = w; f < DFF; f += 4) y += hid[f] * bf2f(w2[f * M + m]);
  red[0][w][threadIdx.x & 63] = s; red[1][w][threadIdx.x & 63] = y; __syncthreads();
  if (w == 0) { int l = threadIdx.x; s = red[0][0][l] + red[0][1][l] + red[0][2][l] + red[0][3][l]; y = red[1][0][l] + red[1][1][l] + red[1][2][l] + red[1][3][l];
    float g = y / (1.f + __expf(-y)); out[m] = __float2bfloat16(bf2f(x[m]) + s * g); }
}

#define WHICH 3
#define KMUL 0

typedef unsigned v4u __attribute__((ext_vector_type(4)));
__device__ __forceinline__ unsigned long long rt() { return __builtin_amdgcn_s_memrealtime(); }
struct PP { const v4u* p[7]; };
__global__ void __launch_bounds__(512) kprobe(const v4u* ckv, const v4u* wq, const v4u* wuk, const v4u* wuv, const v4u* wo, const v4u* w1, const v4u* w2, unsigned long long* ts, unsigned* sink) {
  extern __shared__ char lds[];
  unsigned long long t0 = rt();
  const int t = threadIdx.x, b = blockIdx.x;
  v4u v[21];
  v[0] = wq[b * 1280 + t]; v[1] = wq[b * 1280 + 512 + t]; v[2] = wq[b * 1280 + 1024 + (t & 255)];
  v[3] = wuk[b * 1024 + t]; v[4] = wuk[b * 1024 + 512 + t];
#pragma unroll
  for (int i = 0; i < 8; ++i) v[5 + i] = ckv[b * 4096 + i * 512 + t];
  v[13] = wuv[b * 1024 + t]; v[14] = wuv[b * 1024 + 512 + t];
  v[15] = wo[b * 1280 + t]; v[16] = wo[b * 1280 + 512 + t]; v[17] = wo[b * 1280 + 1024 + (t & 255)];
  v[18] = w1[b * 540 + t]; v[19] = w2[b * 540 + t]; v[20] = (t < 28) ? w1[b * 540 + 512 + t] : w2[b * 540 + 512 + (t % 28)];
  asm volatile("s_waitcnt vmcnt(20)" ::: "memory");
  unsigned long long t1 = rt();
  asm volatile("s_waitcnt vmcnt(8)" ::: "memory");
  unsigned long long t2 = rt();
  asm volatile("s_waitcnt vmcnt(0)" ::: "memory");
  unsigned long long t3 = rt();
  v4u a = v[0];
#pragma unroll
  for (int i = 1; i < 21; ++i) a ^= v[i];
  unsigned r = a.x ^ a.y ^ a.z ^ a.w;
  if (r == 0x12345678) sink[threadIdx.x] = r;
  if ((t & 63) == 0) { unsigned long long* o = ts + ((size_t)b * 8 + (t >> 6)) * 4; o[0] = t0; o[1] = t1; o[2] = t2; o[3] = t3; }
}
__global__ void kspin(const unsigned long long* ts) {
  unsigned long long mn = ~0ull, mx = 0;
  for (int i = threadIdx.x; i < 2048; i += 64) { unsigned long long a = ts[i * 4]; mn = a < mn ? a : mn; unsigned long long e = ts[i * 4 + WHICH]; mx = e > mx ? e : mx; }
  for (int o = 32; o; o >>= 1) { unsigned long long a = __shfl_xor(mn, o), e = __shfl_xor(mx, o); mn = a < mn ? a : mn; mx = e > mx ? e : mx; }
  unsigned long long d = (mx - mn) * KMUL; if (d > 1000000) d = 1000000;
  unsigned long long s = rt(); while (rt() - s < d) __builtin_amdgcn_s_sleep(4);
}

extern "C" void kernel_launch(void* const* d_in, const int* in_sizes, int n_in, void* d_out, int out_size, void* d_ws, size_t ws_size, hipStream_t stream) {
  const unsigned short* x = (const unsigned short*)d_in[0];
  const unsigned short* ckv = (const unsigned short*)d_in[1];
  const unsigned short* wq = (const unsigned short*)d_in[2];
  const unsigned short* wuk = (const unsigned short*)d_in[3];
  const unsigned short* wuv = (const unsigned short*)d_in[4];
  const unsigned short* wo = (const unsigned short*)d_in[5];
  const unsigned short* w1 = (const unsigned short*)d_in[7];
  const unsigned short* w2 = (const unsigned short*)d_in[8];
  float* ws = (float*)d_ws;
  float* q = ws; float* hid = ws + 4096; float* qabs = ws + 8192; float* ctx = ws + 8192 + 16384; float* sc = ws + 65536; float* part = sc + H * SK;
  unsigned long long* ts = (unsigned long long*)((char*)d_ws + (128u << 20));
  hipFuncSetAttribute((const void*)kprobe, hipFuncAttributeMaxDynamicSharedMemorySize, 96 * 1024);
  kprobe<<<256, 512, 96 * 1024, stream>>>((const v4u*)ckv, (const v4u*)wq, (const v4u*)wuk, (const v4u*)wuv, (const v4u*)wo, (const v4u*)w1, (const v4u*)w2, ts, (unsigned*)(ts + 65536));
  k1<<<(H * D + DFF + 63) / 64, 64, 0, stream>>>(x, wq, w1, q, hid);
  k2<<<H * DC / 64, 64, 0, stream>>>(q, wuk, qabs);
  k3<<<SK * H / 256, 256, 0, stream>>>(qabs, ckv, sc);
  k4<<<H, 1024, 0, stream>>>(sc);
  k5<<<64 * H, 512, 0, stream>>>(sc, ckv, part);
  k6<<<H, 512, 0, stream>>>(part, wuv, ctx);
  k7<<<M / 64, 256, 0, stream>>>(x, ctx, wo, hid, w2, (__hip_bfloat16*)d_out);
  kspin<<<1, 64, 0, stream>>>(ts);
}
```

```cpp
#include <hip/hip_runtime.h>

typedef unsigned u32;
typedef unsigned long long u64;
typedef u32 v4u __attribute__((ext_vector_type(4)));
typedef u32 v2u __attribute__((ext_vector_type(2)));
typedef float v4f __attribute__((ext_vector_type(4)));
typedef float v2f __attribute__((ext_vector_type(2)));
typedef __bf16 bfx8 __attribute__((ext_vector_type(8)));
typedef __bf16 bfx2 __attribute__((ext_vector_type(2)));
typedef short v4s __attribute__((ext_vector_type(4)));
typedef __amdgpu_buffer_rsrc_t rsrc_t;
#define LAS __attribute__((address_space(3)))
#define DEVI __device__ __forceinline__

#define W0 12
#define W1 40
#define N0 8
#define NE 10
#define POLL2_SLEEP 8
#define AUX_CKV ""

constexpr u32 WS_QA = 4096, WS_IN3 = 1u << 20, WS_IN5S = 6u << 20, WS_IN5Y = 7u << 20, WS_BYTES = 8u << 20;
constexpr u32 WS_DBG = 16u << 20;
constexpr u32 L_CKV = 0, L_QA = 131072, L_P = 147456, L_XN = 151552, L_QP = 152832, L_QB = 154880, L_MX = 155904, L_LS = 156160,
              L_CP = 156416, L_ML = 160512, L_FLAG = 160576, L_FIN = 160640, L_END = 161664;
constexpr u32 SPIN_CAP = 200000;

DEVI rsrc_t mk(const void* p, u32 bytes) { return __builtin_amdgcn_make_buffer_rsrc(const_cast<void*>(p), 0, bytes, 0x00020000); }
DEVI v4u ld16(rsrc_t r, u32 v, u32 s, int aux) {
  return aux == 16 ? __builtin_amdgcn_raw_buffer_load_b128(r, v, s, 16) : aux == 2 ? __builtin_amdgcn_raw_buffer_load_b128(r, v, s, 2) : __builtin_amdgcn_raw_buffer_load_b128(r, v, s, 0);
}
DEVI void st16(v4u d, rsrc_t r, u32 v) { __builtin_amdgcn_raw_buffer_store_b128(d, r, v, 0, 16); }
DEVI float bflo(u32 x) { return __builtin_bit_cast(float, x << 16); }
DEVI float bfhi(u32 x) { return __builtin_bit_cast(float, x & 0xffff0000u); }
DEVI u32 pk(float a, float b) { v2f t = {a, b}; return __builtin_bit_cast(u32, __builtin_convertvector(t, bfx2)); }
DEVI float rb(float a) { return bflo(pk(a, a)); }
DEVI u32 fu(float a) { return __builtin_bit_cast(u32, a); }
DEVI float uf(u32 a) { return __builtin_bit_cast(float, a); }
template <int C> DEVI float dpp(float x) { return uf(__builtin_amdgcn_update_dpp(0, fu(x), C, 0xf, 0xf, true)); }
template <int C> DEVI u32 dppu(u32 x) { return __builtin_amdgcn_update_dpp(0, x, C, 0xf, 0xf, true); }
DEVI float rowsum(float x) { x += dpp<0x128>(x); x += dpp<0x124>(x); x += dpp<0x122>(x); x += dpp<0x121>(x); return x; }
DEVI float rowmax(float x) { x = fmaxf(x, dpp<0x128>(x)); x = fmaxf(x, dpp<0x124>(x)); x = fmaxf(x, dpp<0x122>(x)); x = fmaxf(x, dpp<0x121>(x)); return x; }
DEVI float sw32add(float a, float b) { auto p = __builtin_amdgcn_permlane32_swap(fu(a), fu(b), false, false); return uf(p[0]) + uf(p[1]); }
DEVI float sw16add(float a, float b) { auto p = __builtin_amdgcn_permlane16_swap(fu(a), fu(b), false, false); return uf(p[0]) + uf(p[1]); }
DEVI float sw32max(float a) { auto p = __builtin_amdgcn_permlane32_swap(fu(a), fu(a), false, false); return fmaxf(uf(p[0]), uf(p[1])); }
DEVI float sw16max(float a) { auto p = __builtin_amdgcn_permlane16_swap(fu(a), fu(a), false, false); return fmaxf(uf(p[0]), uf(p[1])); }
DEVI float wavesum(float x) { x = rowsum(x); x = sw16add(x, x); return sw32add(x, x); }
DEVI v4f mfma(v4u a, v4u b, v4f c) { return __builtin_amdgcn_mfma_f32_16x16x32_bf16(__builtin_bit_cast(bfx8, a), __builtin_bit_cast(bfx8, b), c, 0, 0, 0); }
DEVI u32 sig(u32 r) { return ((r & 3) << 2) | ((r >> 2) & 3); }
template <class T> DEVI T lds_ld(u32 a) { return *(LAS T*)a; }
template <class T> DEVI void lds_st(u32 a, T v) { *(LAS T*)a = v; }
DEVI v2u trd(u32 a) { return __builtin_bit_cast(v2u, __builtin_amdgcn_ds_read_tr16_b64_v4i16((LAS v4s*)a)); }
DEVI void bar() { asm volatile("s_waitcnt lgkmcnt(0)\n\ts_barrier" ::: "memory"); }
DEVI void dma16(rsrc_t r, u32 voff, u32 soff, u32 ldsdst) {
  asm volatile("s_mov_b32 m0, %3\n\ts_nop 0\n\tbuffer_load_dwordx4 %0, %1, %2 offen lds" AUX_CKV ::"v"(voff), "s"(r), "s"(soff), "s"(ldsdst) : "memory");
}
#define SB() __builtin_amdgcn_sched_barrier(0)

#define STAMP(i)

__global__ void __launch_bounds__(512, 2)
mla_decode_kernel(const void* __restrict__ px, const void* __restrict__ pckv, const void* __restrict__ pwq, const void* __restrict__ pwuk,
                  const void* __restrict__ pwuv, const void* __restrict__ pwo, const void* __restrict__ pw1, const void* __restrict__ pw2,
                  void* __restrict__ pout, void* __restrict__ pws) {
  __shared__ __attribute__((aligned(1024))) char smem[L_END];
  asm volatile("" ::"v"((LAS char*)smem));
  STAMP(0);
  const u32 tid = threadIdx.x, lane = tid & 63, n = lane & 15, g = lane >> 4;
  const u32 w = __builtin_amdgcn_readfirstlane(tid >> 6);
  const u32 b = blockIdx.x, xcd = b & 7, r = b >> 3;
  const u32 hgP = r & 1, ks = xcd | ((r >> 1) << 3);
  const u32 hC = (1 - hgP) * 16 + xcd * 2 + ((r >> 1) & 1), j = r >> 2;
  const u32 fb = xcd + 8 * (r >> 3);
  const bool ffn = fb < 27;
  const u32 f0 = fb * 64 + (r & 7) * 8;
  const bool isA = w < 4;
  const rsrc_t rws = mk(pws, WS_BYTES);
  const rsrc_t rwo = mk(pwo, 32u * 128 * 640 * 2);
  const rsrc_t rx = mk(px, 1280);
  u32 msk[4];
#pragma unroll
  for (int q = 0; q < 4; ++q) msk[q] = (q == (int)((n & 7) >> 1)) ? ((n & 1) ? 0xffff0000u : 0x0000ffffu) : 0u;

  v4u wo[20];
  v4u w1r[10], w2r[10];
  u32 xnp[5];
  v2u ep;
  const u32 dblk = (w + j) & 7;
  const u32 wo_v = ((hC * 128 + dblk * 16 + 4 * g) * 640 + n * 8) * 2;
  v4f sacc[2];

  if (isA) {
    const u32 a = w;
    const rsrc_t rwq = mk(pwq, 640u * 4096 * 2), rwuk = mk(pwuk, 512u * 4096 * 2);
    ep = __builtin_amdgcn_raw_buffer_load_b64(rws, 0, 0, 16);
    v4u xa = ld16(rx, lane * 20, 0, 0);
    u32 xb = __builtin_amdgcn_raw_buffer_load_b32(rx, lane * 20 + 16, 0, 0);
    v4u wq[40];
    const u32 wq_v = (4 * a + g) * 8192 + hC * 256 + n * 16;
    auto wq_s = [&](int t) { u32 tt = t + 5 * j; tt = tt >= 40 ? tt - 40 : tt; return tt * 131072; };
#pragma unroll
    for (int t = 0; t < W0; ++t) wq[t] = ld16(rwq, wq_v, wq_s(t), 0);
    SB();
    {
      const v4u pois = {~0u, ~0u, ~0u, ~0u};
#pragma unroll
      for (int i = 0; i < 4; ++i) st16(pois, rws, WS_IN3 + (hC * 8 + j) * 16384 + (i * 4 + a) * 1024 + lane * 16);
      if (tid < 160) {
        const u32 o = ((tid >> 2) * 256 + b) * 64 + (tid & 3) * 16;
        st16(pois, rws, WS_IN5S + o);
        const u32 yv = ffn ? ~0u : 0u;
        st16((v4u){yv, yv, yv, yv}, rws, WS_IN5Y + o);
      }
    }
    if (tid == 0) lds_st<v4u>(L_FLAG, (v4u){0, 0, 0, 0});
    bar();
    SB();
    float xv[10] = {bflo(xa.x), bfhi(xa.x), bflo(xa.y), bfhi(xa.y), bflo(xa.z), bfhi(xa.z), bflo(xa.w), bfhi(xa.w), bflo(xb), bfhi(xb)};
    float s1 = 0.f;
#pragma unroll
    for (int i = 0; i < 10; ++i) s1 += xv[i];
    const float mean = wavesum(s1) * (1.0f / 640.0f);
    float s2 = 0.f;
#pragma unroll
    for (int i = 0; i < 10; ++i) { float d = xv[i] - mean; s2 += d * d; }
    const float var = wavesum(s2) * (1.0f / 640.0f);
    const float mb = rb(mean), sd = rb(__builtin_amdgcn_sqrtf(rb(rb(var) + 1e-5f)));
    const float rs = __builtin_amdgcn_rcpf(sd);
#pragma unroll
    for (int i = 0; i < 5; ++i) {
      u32 dp = pk(xv[2 * i] - mb, xv[2 * i + 1] - mb);
      xnp[i] = pk(bflo(dp) * rs, bfhi(dp) * rs);
    }
#pragma unroll
    for (int i = 0; i < 10; ++i) {
      u32 m = lane * 10 + i, t = (m >> 4) + 40 - 5 * j;
      t = t >= 40 ? t - 40 : t;
      lds_st<unsigned short>(L_XN + ((m & 15) * 40 + t) * 2, (unsigned short)((i & 1) ? xnp[i >> 1] >> 16 : xnp[i >> 1]));
    }
    v4u xr[5];
#pragma unroll
    for (int i = 0; i < 5; ++i) xr[i] = lds_ld<v4u>(L_XN + (4 * a + g) * 80 + i * 16);
    STAMP(1);
    SB();
#pragma unroll
    for (int t = W0; t < W1; ++t) wq[t] = ld16(rwq, wq_v, wq_s(t), 0);
    SB();
    if (W1 < 40) { if (lane == 0) __hip_atomic_fetch_add((LAS u32*)L_FLAG, 1u, __ATOMIC_RELAXED, __HIP_MEMORY_SCOPE_WORKGROUP); SB(); }
#pragma unroll
    for (int t = W1; t < 40; ++t) wq[t] = ld16(rwq, wq_v, wq_s(t), 0);
    v4u wuk[4];
#pragma unroll
    for (int u = 0; u < 4; ++u) wuk[u] = ld16(rwuk, (j * 64 + 16 * a + n) * 8192 + hC * 256 + g * 16 + u * 64, 0, 2);
    SB();
    if (W1 >= 40) { if (lane == 0) __hip_atomic_fetch_add((LAS u32*)L_FLAG, 1u, __ATOMIC_RELAXED, __HIP_MEMORY_SCOPE_WORKGROUP); SB(); }
    STAMP(2);
    u32 selE[4], selO[4];
#pragma unroll
    for (int q = 0; q < 4; ++q) { selE[q] = (0x01000100u & msk[q]) | (0x0c0c0c0cu & ~msk[q]); selO[q] = (0x03020302u & msk[q]) | (0x0c0c0c0cu & ~msk[q]); }
    v4f qa = {0.f, 0.f, 0.f, 0.f};
#pragma unroll
    for (int t = 0; t < 40; ++t) {
      const u32 xp = xr[t >> 3][(t >> 1) & 3];
      v4u af;
#pragma unroll
      for (int q = 0; q < 4; ++q) af[q] = __builtin_amdgcn_perm(xp, xp, (t & 1) ? selO[q] : selE[q]);
      qa = mfma(af, wq[t], qa);
    }
    if (g < 2) lds_st<v4f>(L_QP + a * 512 + (n * 8 + g * 4) * 4, qa);
    asm volatile("s_waitcnt lgkmcnt(0)" ::: "memory");
    if (lane == 0) __hip_atomic_fetch_add((LAS u32*)(L_FLAG + 4), 1u, __ATOMIC_RELAXED, __HIP_MEMORY_SCOPE_WORKGROUP);
    STAMP(3);
    _Pragma("nounroll") for (u32 sp = 0; sp < SPIN_CAP; ++sp) {
      u32 c = __hip_atomic_load((LAS u32*)(L_FLAG + 4), __ATOMIC_RELAXED, __HIP_MEMORY_SCOPE_WORKGROUP);
      if (__builtin_amdgcn_readfirstlane(c) >= 4) break;
    }
    asm volatile("" ::: "memory");
    v2f qs = lds_ld<v2f>(L_QP + lane * 8) + lds_ld<v2f>(L_QP + 512 + lane * 8) + lds_ld<v2f>(L_QP + 1024 + lane * 8) + lds_ld<v2f>(L_QP + 1536 + lane * 8);
    lds_st<u32>(L_QB + a * 256 + lane * 4, pk(qs.x, qs.y));
    v4f qb = {0.f, 0.f, 0.f, 0.f};
#pragma unroll
    for (int u = 0; u < 4; ++u) qb = mfma(wuk[u], lds_ld<v4u>(L_QB + a * 256 + u * 64 + g * 16), qb);
    const float sc = 0.08838834764831845f * 1.4426950408889634f;
    const u64 tag = (((u64)ep.y << 32) | ep.x) + 1;
    const u32 tlo = (u32)tag, thi = (u32)(tag >> 32);
    asm volatile("s_waitcnt vmcnt(0)" ::: "memory");
    if (n == 0) st16((v4u){pk(qb.x * sc, qb.y * sc), pk(qb.z * sc, qb.w * sc), tlo, thi}, rws, WS_QA + ((hC * 8 + j) * 16 + 4 * a + g) * 16);
    STAMP(4);
    __builtin_amdgcn_s_sleep(POLL2_SLEEP);
    v4u qv[8];
    _Pragma("nounroll") for (u32 sp = 0;; ++sp) {
      asm volatile("" ::: "memory");
#pragma unroll
      for (int i = 0; i < 8; ++i) qv[i] = ld16(rws, WS_QA + hgP * 32768 + (a * 8 + i) * 1024 + lane * 16, 0, 16);
      bool ok = true;
#pragma unroll
      for (int i = 0; i < 8; ++i) ok &= (qv[i].z == tlo) & (qv[i].w == thi);
      if (__all(ok) || sp >= SPIN_CAP) break;
    }
#pragma unroll
    for (int i = 0; i < 8; ++i) {
      const u32 h = a * 4 + (i >> 1), kap = (i & 1) * 32 + (lane >> 1);
      lds_st<v2u>(L_QA + h * 1024 + (kap >> 4) * 256 + (((kap & 15) ^ sig(h)) << 4) + (lane & 1) * 8, (v2u){qv[i].x, qv[i].y});
    }
    STAMP(5);
    bar();
    SB();
    if (w == 0 && ffn) {
      const rsrc_t rw1 = mk(pw1, 640u * 1728 * 2), rw2 = mk(pw2, 1728u * 640 * 2);
#pragma unroll
      for (int i = 0; i < 10; ++i) w1r[i] = ld16(rw1, ((lane * 10) * 1728 + f0) * 2, i * 3456, 0);
#pragma unroll
      for (int i = 0; i < 10; ++i) w2r[i] = ld16(rw2, ((f0 + g) * 640 + n * 8) * 2, ((i / 5) * 4 * 640 + (i % 5) * 128) * 2, 2);
    }
#pragma unroll
    for (int i = 0; i < NE; ++i) wo[i] = ld16(rwo, wo_v, ((i / 5) * 640 + (i % 5) * 128) * 2, 0);
    SB();
    STAMP(6);
    bar();
  } else {
    const u32 l = w - 4;
    const rsrc_t rck = mk(pckv, 16384u * 1024);
    const u32 sbase = (ks * 128 + 32 * l) * 1024;
#pragma unroll
    for (int i = 0; i < N0; ++i) dma16(rck, (lane * 16) ^ (sig(i & 15) << 4), sbase + i * 1024, L_CKV + (32 * l + i) * 1024);
    bar();
    _Pragma("nounroll") for (u32 sp = 0; sp < SPIN_CAP; ++sp) {
      u32 c = __hip_atomic_load((LAS u32*)L_FLAG, __ATOMIC_RELAXED, __HIP_MEMORY_SCOPE_WORKGROUP);
      if (__builtin_amdgcn_readfirstlane(c) >= 4) break;
      __builtin_amdgcn_s_sleep(1);
    }
    STAMP(1);
#pragma unroll
    for (int i = N0; i < 32; ++i) dma16(rck, (lane * 16) ^ (sig(i & 15) << 4), sbase + i * 1024, L_CKV + (32 * l + i) * 1024);
    STAMP(2);
    bar();
    STAMP(3);
    v4u qf[16];
    u32 ra[4];
#pragma unroll
    for (int u = 0; u < 4; ++u) ra[u] = n * 1024 + (((g * 4 + u) ^ sig(n)) << 4);
#pragma unroll
    for (int u = 0; u < 16; ++u) qf[u] = lds_ld<v4u>(L_QA + ra[u & 3] + (u >> 2) * 256);
#pragma unroll
    for (int tt = 0; tt < 2; ++tt) {
      if (tt == 0) asm volatile("s_waitcnt vmcnt(16)" ::: "memory"); else asm volatile("s_waitcnt vmcnt(0)" ::: "memory");
      if (tt == 1) { STAMP(4); }
      v4f acc = {0.f, 0.f, 0.f, 0.f};
      v4u kf[16];
#pragma unroll
      for (int u = 0; u < 16; ++u) kf[u] = lds_ld<v4u>(L_CKV + (32 * l + 16 * tt) * 1024 + ra[u & 3] + (u >> 2) * 256);
      SB();
#pragma unroll
      for (int u = 0; u < 16; ++u) acc = mfma(kf[u], qf[u], acc);
      SB();
      sacc[tt] = acc;
    }
    float mx = fmaxf(fmaxf(fmaxf(sacc[0].x, sacc[0].y), fmaxf(sacc[0].z, sacc[0].w)), fmaxf(fmaxf(sacc[1].x, sacc[1].y), fmaxf(sacc[1].z, sacc[1].w)));
    mx = sw32max(sw16max(mx));
    if (g == 0) lds_st<float>(L_MX + n * 16 + l * 4, mx);
    STAMP(6);
    bar();
  }
  STAMP(7);
  const v4f m4 = lds_ld<v4f>(L_MX + n * 16);
  const float mrow = fmaxf(fmaxf(m4.x, m4.y), fmaxf(m4.z, m4.w));
  if (!isA) {
    const u32 l = w - 4;
    float ls = 0.f;
#pragma unroll
    for (int tt = 0; tt < 2; ++tt) {
      float p0 = __builtin_amdgcn_exp2f(sacc[tt].x - mrow), p1 = __builtin_amdgcn_exp2f(sacc[tt].y - mrow);
      float p2 = __builtin_amdgcn_exp2f(sacc[tt].z - mrow), p3 = __builtin_amdgcn_exp2f(sacc[tt].w - mrow);
      ls += (p0 + p1) + (p2 + p3);
      lds_st<v2u>(L_P + n * 256 + (((4 * l + 2 * tt + (g >> 1)) ^ n) << 4) + (g & 1) * 8, (v2u){pk(p0, p1), pk(p2, p3)});
    }
    ls = sw16add(ls, ls); ls = sw32add(ls, ls);
    if (g == 0) lds_st<float>(L_LS + n * 16 + l * 4, ls);
  }
  v4u vt[4][4];
  {
    const u32 q = n >> 2, p = n & 3;
#pragma unroll
    for (int kk = 0; kk < 4; ++kk)
#pragma unroll
      for (int ct = 0; ct < 4; ++ct) {
        v2u h2[2];
#pragma unroll
        for (int t = 0; t < 2; ++t) {
          const u32 row = 8 * g + 4 * t + q;
          h2[t] = trd(L_CKV + (32 * kk + row) * 1024 + (w >> 1) * 256 + ((((w & 1) * 8 + 2 * ct + (p >> 1)) ^ sig(row & 15)) << 4) + 8 * (p & 1));
        }
        vt[kk][ct] = (v4u){h2[0].x, h2[0].y, h2[1].x, h2[1].y};
      }
  }
  bar();
  STAMP(8);
  {
    v4f o[4];
#pragma unroll
    for (int ct = 0; ct < 4; ++ct) o[ct] = (v4f){0.f, 0.f, 0.f, 0.f};
    const v4f l4 = lds_ld<v4f>(L_LS + n * 16);
    v4u pf[4];
#pragma unroll
    for (int kk = 0; kk < 4; ++kk) pf[kk] = lds_ld<v4u>(L_P + n * 256 + (((4 * kk + g) ^ n) << 4));
    SB();
#pragma unroll
    for (int kk = 0; kk < 4; ++kk)
#pragma unroll
      for (int ct = 0; ct < 4; ++ct) o[ct] = mfma(vt[kk][ct], pf[kk], o[ct]);
    const float lsum = (l4.x + l4.y) + (l4.z + l4.w);
    const float inv = __builtin_amdgcn_rcpf(lsum);
    const u32 lse = fu(mrow + __builtin_amdgcn_logf(lsum));
#pragma unroll
    for (int s = 0; s < 2; ++s) {
      const u32 bits = lse >> (16 * s + 4 * g);
      v4u d;
#pragma unroll
      for (int q = 0; q < 4; ++q) {
        const v4f ov = o[2 * s + (q >> 1)];
        u32 x = (q & 1) ? pk(ov.z * inv, ov.w * inv) : pk(ov.x * inv, ov.y * inv);
        x = min(x, 0xfffeffffu);
        d[q] = (x & ~1u) | ((bits >> q) & 1u);
      }
      st16(d, rws, WS_IN3 + (((hgP * 16 + n) * 8 + w) * 128 + ks) * 128 + s * 64 + g * 16);
    }
  }
  SB();
  STAMP(9);
  {
    const rsrc_t rwuv = mk(pwuv, 512u * 4096 * 2);
#pragma unroll
    for (int i = 0; i < 2; ++i) {
      const u32 row = 8 * w + 4 * i + g;
      dma16(rwuv, (j * 64 + row) * 8192 + hC * 256 + ((n ^ sig(row & 15)) << 4), 0, L_QA + (8 * w + 4 * i) * 256);
    }
    if (!isA) {
#pragma unroll
      for (int i = 0; i < NE; ++i) wo[i] = ld16(rwo, wo_v, ((i / 5) * 640 + (i % 5) * 128) * 2, 0);
    }
#pragma unroll
    for (int i = NE; i < 20; ++i) wo[i] = ld16(rwo, wo_v, ((i / 5) * 640 + (i % 5) * 128) * 2, 0);
  }
  SB();
  if (w == 0 && ffn) {
    float ha[8];
#pragma unroll
    for (int e = 0; e < 8; ++e) ha[e] = 0.f;
#pragma unroll
    for (int i = 0; i < 10; ++i) {
      const float xn = (i & 1) ? bfhi(xnp[i >> 1]) : bflo(xnp[i >> 1]);
#pragma unroll
      for (int q = 0; q < 4; ++q) { ha[2 * q] += xn * bflo(w1r[i][q]); ha[2 * q + 1] += xn * bfhi(w1r[i][q]); }
    }
    float h0 = sw16add(sw32add(ha[0], ha[2]), sw32add(ha[1], ha[3]));
    float h1 = sw16add(sw32add(ha[4], ha[6]), sw32add(ha[5], ha[7]));
    h0 = fmaxf(rowsum(h0), 0.f); h1 = fmaxf(rowsum(h1), 0.f);
    const u32 hh0 = pk(h0, h0), hh1 = pk(h1, h1);
    const v4u a0 = {hh0 & msk[0], hh0 & msk[1], hh0 & msk[2], hh0 & msk[3]}, a1 = {hh1 & msk[0], hh1 & msk[1], hh1 & msk[2], hh1 & msk[3]};
#pragma unroll
    for (int mb = 0; mb < 5; ++mb) {
      v4f y = mfma(a0, w2r[mb], (v4f){0.f, 0.f, 0.f, 0.f});
      y = mfma(a1, w2r[5 + mb], y);
      if (g < 2) st16((v4u){min(fu(y.x), ~1u), min(fu(y.y), ~1u), min(fu(y.z), ~1u), min(fu(y.w), ~1u)}, rws,
                      WS_IN5Y + ((mb * 8 + (n >> 1)) * 256 + b) * 64 + (n & 1) * 32 + g * 16);
    }
  }
  SB();
  STAMP(10);
  v4u pv[2];
  _Pragma("nounroll") for (u32 sp = 0;; ++sp) {
      asm volatile("" ::: "memory");
#pragma unroll
    for (int i = 0; i < 2; ++i) pv[i] = ld16(rws, WS_IN3 + (hC * 8 + j) * 16384 + (i * 8 + w) * 1024 + lane * 16, 0, 16);
    u32 all = 0;
#pragma unroll
    for (int i = 0; i < 2; ++i) all = max(all, max(max(pv[i].x, pv[i].y), max(pv[i].z, pv[i].w)));
    if (__all(all != ~0u) || sp >= SPIN_CAP) break;
  }
  STAMP(11);
  {
    float ls[2];
#pragma unroll
    for (int i = 0; i < 2; ++i) {
      u32 nib = (pv[i].x & 1) | ((pv[i].y & 1) << 1) | ((pv[i].z & 1) << 2) | ((pv[i].w & 1) << 3);
      nib <<= 4 * (lane & 7);
      nib |= dppu<0xb1>(nib); nib |= dppu<0x4e>(nib); nib |= dppu<0x141>(nib);
      ls[i] = uf(nib);
    }
    float mw = fmaxf(ls[0], ls[1]);
    mw = fmaxf(mw, dpp<0x128>(mw));
    mw = sw32max(sw16max(mw));
    v4f c = {0.f, 0.f, 0.f, 0.f};
    float lw = 0.f;
#pragma unroll
    for (int i = 0; i < 2; ++i) {
      const float e = __builtin_amdgcn_exp2f(ls[i] - mw);
      const u32 ee = pk(e, e);
      lw += bflo(ee);
      c = mfma((v4u){ee & msk[0], ee & msk[1], ee & msk[2], ee & msk[3]}, pv[i], c);
    }
    lw += dpp<0x128>(lw);
    lw = sw16add(lw, lw); lw = sw32add(lw, lw);
    if ((g >> 1) == (n >> 3)) lds_st<v4f>(L_CP + (w * 2 + (n >> 3)) * 256 + ((((n & 7) * 2 + (g & 1)) ^ (w * 2 + (n >> 3))) << 4), c);
    if (lane == 0) lds_st<v2f>(L_ML + w * 8, (v2f){mw, lw});
  }
  asm volatile("s_waitcnt vmcnt(0)" ::: "memory");
  bar();
  STAMP(12);
  {
    const v2f ml = lds_ld<v2f>(L_ML + (n >> 1) * 8);
    v4f cp[4];
#pragma unroll
    for (int i = 0; i < 4; ++i) cp[i] = lds_ld<v4f>(L_CP + n * 256 + ((((i >> 1) * 8 + g * 2 + (i & 1)) ^ n) << 4));
    v4u uv[2];
    {
      const u32 q = n >> 2, p = n & 3;
#pragma unroll
      for (int u = 0; u < 2; ++u) {
        v2u h2[2];
#pragma unroll
        for (int t = 0; t < 2; ++t) {
          const u32 row = 32 * u + 16 * t + 4 * g + q;
          h2[t] = trd(L_QA + row * 256 + (((2 * dblk + (p >> 1)) ^ sig(row & 15)) << 4) + 8 * (p & 1));
        }
        uv[u] = (v4u){h2[0].x, h2[0].y, h2[1].x, h2[1].y};
      }
    }
    const float mg = rowmax(ml.x);
    const float e = __builtin_amdgcn_exp2f(ml.x - mg);
    const float den = rowsum(e * ml.y) * 0.5f;
    const float sc = e * __builtin_amdgcn_rcpf(den);
    v4f cx = {0.f, 0.f, 0.f, 0.f};
#pragma unroll
    for (int u = 0; u < 2; ++u) {
      const v4f c0 = cp[2 * u] * sc, c1 = cp[2 * u + 1] * sc;
      cx = mfma(uv[u], (v4u){pk(c0.x, c0.y), pk(c0.z, c0.w), pk(c1.x, c1.y), pk(c1.z, c1.w)}, cx);
    }
    v4f sa[5];
#pragma unroll
    for (int mb = 0; mb < 5; ++mb) sa[mb] = (v4f){0.f, 0.f, 0.f, 0.f};
#pragma unroll
    for (int t = 0; t < 4; ++t) {
      const float cv = rowsum(cx[t]);
      const u32 cc = pk(cv, cv);
      const v4u af = {cc & msk[0], cc & msk[1], cc & msk[2], cc & msk[3]};
#pragma unroll
      for (int mb = 0; mb < 5; ++mb) sa[mb] = mfma(af, wo[t * 5 + mb], sa[mb]);
    }
    if (g < 2) {
#pragma unroll
      for (int mb = 0; mb < 5; ++mb) lds_st<v4f>(L_CKV + w * 2560 + (mb * 128 + n * 8 + g * 4) * 4, sa[mb]);
    }
  }
  bar();
  if (tid < 160) {
    v4f s = lds_ld<v4f>(L_CKV + tid * 16);
#pragma unroll
    for (int i = 1; i < 8; ++i) s += lds_ld<v4f>(L_CKV + i * 2560 + tid * 16);
    st16((v4u){min(fu(s.x), ~1u), min(fu(s.y), ~1u), min(fu(s.z), ~1u), min(fu(s.w), ~1u)}, rws, WS_IN5S + ((tid >> 2) * 256 + b) * 64 + (tid & 3) * 16);
  }
  STAMP(13);
  if (!ffn) {
    const u32 rid = (xcd - 3) * 8 + (r & 7);
    u32 xo = 0;
    if (tid < 16) xo = __builtin_amdgcn_raw_buffer_load_b16(rx, (rid * 16 + tid) * 2, 0, 0);
    v4u sv[2], yv[2];
    _Pragma("nounroll") for (u32 sp = 0;; ++sp) {
      asm volatile("" ::: "memory");
#pragma unroll
      for (int i = 0; i < 2; ++i) yv[i] = ld16(rws, WS_IN5Y + rid * 16384 + (i * 8 + w) * 1024 + lane * 16, 0, 16);
#pragma unroll
      for (int i = 0; i < 2; ++i) sv[i] = ld16(rws, WS_IN5S + rid * 16384 + (i * 8 + w) * 1024 + lane * 16, 0, 16);
      u32 all = 0;
#pragma unroll
      for (int i = 0; i < 2; ++i) all = max(max(all, max(max(sv[i].x, sv[i].y), max(sv[i].z, sv[i].w))), max(max(yv[i].x, yv[i].y), max(yv[i].z, yv[i].w)));
      if (__all(all != ~0u) || sp >= SPIN_CAP) break;
    }
    STAMP(14);
    float v[4];
#pragma unroll
    for (int k = 0; k < 4; ++k) v[k] = sw32add(uf(sv[0][k]) + uf(sv[1][k]), uf(yv[0][k]) + uf(yv[1][k]));
    float r0 = sw16add(v[0], v[1]), r1 = sw16add(v[2], v[3]);
    r0 += dpp<0x128>(r0); r0 += dpp<0x124>(r0);
    r1 += dpp<0x128>(r1); r1 += dpp<0x124>(r1);
    if (n < 4) {
      const u32 col = n * 4 + (g & 1);
      lds_st<float>(L_FIN + (((g >> 1) * 16 + col) * 8 + w) * 4, r0);
      lds_st<float>(L_FIN + (((g >> 1) * 16 + col + 2) * 8 + w) * 4, r1);
    }
    bar();
    if (tid < 16) {
      const v4f s0 = lds_ld<v4f>(L_FIN + tid * 32), s1 = lds_ld<v4f>(L_FIN + tid * 32 + 16);
      const v4f y0 = lds_ld<v4f>(L_FIN + 512 + tid * 32), y1 = lds_ld<v4f>(L_FIN + 512 + tid * 32 + 16);
      const float s = rb(((s0.x + s0.y) + (s0.z + s0.w)) + ((s1.x + s1.y) + (s1.z + s1.w)));
      const float y = rb(((y0.x + y0.y) + (y0.z + y0.w)) + ((y1.x + y1.y) + (y1.z + y1.w)));
      const float gt = rb(y / (1.0f + __expf(-y)));
      const u32 o = pk(bflo(xo) + rb(s * gt), 0.f);
      ((unsigned short*)pout)[rid * 16 + tid] = (unsigned short)o;
    }
    if (rid == 0 && tid == 0) {
      const u64 tag = (((u64)ep.y << 32) | ep.x) + 1;
      __builtin_amdgcn_raw_buffer_store_b64((v2u){(u32)tag, (u32)(tag >> 32)}, rws, 0, 0, 16);
    }
  }
}

extern "C" void kernel_launch(void* const* d_in, const int* in_sizes, int n_in, void* d_out, int out_size, void* d_ws, size_t ws_size,
                              hipStream_t stream) {
  mla_decode_kernel<<<256, 512, 0, stream>>>(d_in[0], d_in[1], d_in[2], d_in[3], d_in[4], d_in[5], d_in[7], d_in[8], d_out, d_ws);
}
```

```cpp
#include <hip/hip_runtime.h>
#include <type_traits>

typedef unsigned u32;
typedef unsigned long long u64;
typedef u32 v4u __attribute__((ext_vector_type(4)));
typedef u32 v2u __attribute__((ext_vector_type(2)));
typedef float v4f __attribute__((ext_vector_type(4)));
typedef float v2f __attribute__((ext_vector_type(2)));
typedef __bf16 bfx8 __attribute__((ext_vector_type(8)));
typedef __bf16 bfx2 __attribute__((ext_vector_type(2)));
typedef short v4s __attribute__((ext_vector_type(4)));
typedef __amdgpu_buffer_rsrc_t rsrc_t;
#define LAS __attribute__((address_space(3)))
#define DEVI __device__ __forceinline__

#define WD 6
#define UP 10
#define FFP 0
#define UVP 5
#define BD 8
#define CODE_BYTES 16384
#define KD 8
#define S3B 1
#define QW 2
#define FSP 4
#define KSW 1
#define FPOS 12
#define QRS 0
#define OSH 1024
#define OSC 256
#define OSK 32768
#define MLP 1
#define MLW 0
#define QS 0
#define PVS 1
#define FBAR 1
#define KS 8
#define SD 12
#define PF 4
#define BK_SLEEP 16
#define RED_SLEEP 0
#define Y_SLEEP 8
#define ML_SLEEP 4
#define P_SLEEP 0
#define AUX_CKV " nt"

constexpr u32 WS_FL = 2048, WS_QA = 4096, WS_ML = 131072, WS_CS = 262144, WS_IN3 = 1u << 20, WS_IN5 = 6u << 20, WS_BYTES = 8u << 20;
constexpr u32 WS_DBG = 16u << 20;
constexpr u32 L_END = 163840;
constexpr u32 L_CKV = 0, L_QA = 131072, L_MX = 0, L_L = 256, L_P = 8192, L_FF = 16384, L_XK = L_QA, L_PFD = L_QA + 8192;
constexpr u32 L_X = 0, L_UK = 4096, L_WO = 0, L_UV = 122880, L_QP = 155648, L_CP = L_UV, L_ML = L_UV + 4096, L_CX = L_UV + 4352;
constexpr u32 SPIN_CAP = 100000;

DEVI rsrc_t mk(const void* p, u32 bytes) { return __builtin_amdgcn_make_buffer_rsrc(const_cast<void*>(p), 0, bytes, 0x00020000); }
template <int AUX> DEVI v4u ld16(rsrc_t r, u32 v, u32 s) { return __builtin_amdgcn_raw_buffer_load_b128(r, v, s, AUX); }
DEVI void st16(v4u d, rsrc_t r, u32 v, u32 s) {
  __builtin_amdgcn_raw_buffer_store_b128(d, r, v, s, 16);
  asm volatile("s_nop 1" ::"v"(d));
}
DEVI float bflo(u32 x) { return __builtin_bit_cast(float, x << 16); }
DEVI float bfhi(u32 x) { return __builtin_bit_cast(float, x & 0xffff0000u); }
DEVI u32 pk(float a, float b) { v2f t = {a, b}; return __builtin_bit_cast(u32, __builtin_convertvector(t, bfx2)); }
DEVI float rb(float a) { return bfhi(pk(a, a)); }
DEVI u32 fu(float a) { return __builtin_bit_cast(u32, a); }
DEVI float uf(u32 a) { return __builtin_bit_cast(float, a); }
template <int C> DEVI float dpp(float x) { return uf(__builtin_amdgcn_update_dpp(0, fu(x), C, 0xf, 0xf, true)); }
template <int C> DEVI u32 dppu(u32 x) { return __builtin_amdgcn_update_dpp(0, x, C, 0xf, 0xf, true); }
DEVI float rowsum(float x) { x += dpp<0x128>(x); x += dpp<0x124>(x); x += dpp<0x122>(x); x += dpp<0x121>(x); return x; }
DEVI float sw32add(float a, float b) { auto p = __builtin_amdgcn_permlane32_swap(fu(a), fu(b), false, false); return uf(p[0]) + uf(p[1]); }
DEVI float sw16add(float a, float b) { auto p = __builtin_amdgcn_permlane16_swap(fu(a), fu(b), false, false); return uf(p[0]) + uf(p[1]); }
DEVI float sw32max(float a) { auto p = __builtin_amdgcn_permlane32_swap(fu(a), fu(a), false, false); return fmaxf(uf(p[0]), uf(p[1])); }
DEVI float sw16max(float a) { auto p = __builtin_amdgcn_permlane16_swap(fu(a), fu(a), false, false); return fmaxf(uf(p[0]), uf(p[1])); }
DEVI v4f mfma(v4u a, v4u b, v4f c) { return __builtin_amdgcn_mfma_f32_16x16x32_bf16(__builtin_bit_cast(bfx8, a), __builtin_bit_cast(bfx8, b), c, 0, 0, 0); }
DEVI float dot2(u32 a, u32 b, float c) { return __builtin_amdgcn_fdot2_f32_bf16(__builtin_bit_cast(bfx2, a), __builtin_bit_cast(bfx2, b), c, false); }
DEVI u32 sig(u32 r) { return ((r & 3) << 2) | ((r >> 2) & 3); }
template <class T> DEVI T lds_ld(u32 a) { return *(LAS T*)a; }
template <class T> DEVI void lds_st(u32 a, T v) { *(LAS T*)a = v; }
DEVI v2u trd(u32 a) { return __builtin_bit_cast(v2u, __builtin_amdgcn_ds_read_tr16_b64_v4i16((LAS v4s*)a)); }
DEVI v4u trd2(u32 a, u32 b) { v2u x = trd(a), y = trd(b); return (v4u){x.x, x.y, y.x, y.y}; }
DEVI void bar() { asm volatile("s_waitcnt lgkmcnt(0)\n\ts_barrier" ::: "memory"); }
DEVI void dma16(rsrc_t r, u32 voff, u32 soff, u32 ldsdst) {
  asm volatile("s_mov_b32 m0, %3\n\ts_nop 0\n\tbuffer_load_dwordx4 %0, %1, %2 offen lds" ::"v"(voff), "s"(r), "s"(soff), "s"(ldsdst) : "memory");
}
DEVI void dma16s(rsrc_t r, u32 voff, u32 soff, u32 ldsdst) {
  asm volatile("s_mov_b32 m0, %3\n\ts_nop 0\n\tbuffer_load_dwordx4 %0, %1, %2 offen" AUX_CKV " lds" ::"v"(voff), "s"(r), "s"(soff), "s"(ldsdst) : "memory");
}
DEVI void st8(v2u d, rsrc_t r, u32 v, u32 s) { __builtin_amdgcn_raw_buffer_store_b64(d, r, v, s, 16); }
DEVI v4u ld16a(rsrc_t r, u32 voff, u32 soff) {
  v4u d;
  asm volatile("buffer_load_dwordx4 %0, %1, %2, %3 offen sc1" : "=v"(d) : "v"(voff), "s"(r), "s"(soff) : "memory");
  return d;
}
DEVI v4u ld16p(rsrc_t r, u32 voff, u32 soff) {
  v4u d;
  asm volatile("buffer_load_dwordx4 %0, %1, %2, %3 offen" : "=v"(d) : "v"(voff), "s"(r), "s"(soff) : "memory");
  return d;
}
DEVI v2u ld8a(rsrc_t r, u32 voff, u32 soff) {
  v2u d;
  asm volatile("buffer_load_dwordx2 %0, %1, %2, %3 offen sc1" : "=v"(d) : "v"(voff), "s"(r), "s"(soff) : "memory");
  return d;
}
DEVI u32 ld4a(rsrc_t r, u32 voff, u32 soff) {
  u32 d;
  asm volatile("buffer_load_dword %0, %1, %2, %3 offen sc1" : "=v"(d) : "v"(voff), "s"(r), "s"(soff) : "memory");
  return d;
}
DEVI void ldpoll(v4u* d, u32& t, const u64* m, u64 mt, rsrc_t r, u32 vd, const u32* sd, u32 vt, u32 st) {
  asm volatile("s_mov_b64 exec, %12\n\tbuffer_load_dwordx4 %0, %5, %7, %17 offen sc1\n\t"
               "s_mov_b64 exec, %13\n\tbuffer_load_dwordx4 %1, %5, %7, %9 offen sc1\n\t"
               "s_mov_b64 exec, %14\n\tbuffer_load_dwordx4 %2, %5, %7, %10 offen sc1\n\t"
               "s_mov_b64 exec, %15\n\tbuffer_load_dwordx4 %3, %5, %7, %11 offen sc1\n\t"
               "s_mov_b64 exec, %16\n\tbuffer_load_dword %4, %6, %7, %8 offen sc1\n\t"
               "s_mov_b64 exec, -1\n\ts_waitcnt vmcnt(0)"
               : "+v"(d[0]), "+v"(d[1]), "+v"(d[2]), "+v"(d[3]), "+v"(t)
               : "v"(vd), "v"(vt), "s"(r), "s"(st), "s"(sd[1]), "s"(sd[2]), "s"(sd[3]), "s"(m[0]), "s"(m[1]), "s"(m[2]), "s"(m[3]), "s"(mt), "s"(sd[0])
               : "memory");
}
DEVI void ldpoll4(v4u* d, const u64* m, rsrc_t r, u32 vd, const u32* sd) {
  asm volatile("s_mov_b64 exec, %10\n\tbuffer_load_dwordx4 %0, %4, %5, %6 offen sc1\n\t"
               "s_mov_b64 exec, %11\n\tbuffer_load_dwordx4 %1, %4, %5, %7 offen sc1\n\t"
               "s_mov_b64 exec, %12\n\tbuffer_load_dwordx4 %2, %4, %5, %8 offen sc1\n\t"
               "s_mov_b64 exec, %13\n\tbuffer_load_dwordx4 %3, %4, %5, %9 offen sc1\n\t"
               "s_mov_b64 exec, -1\n\ts_waitcnt vmcnt(0)"
               : "+v"(d[0]), "+v"(d[1]), "+v"(d[2]), "+v"(d[3])
               : "v"(vd), "s"(r), "s"(sd[0]), "s"(sd[1]), "s"(sd[2]), "s"(sd[3]), "s"(m[0]), "s"(m[1]), "s"(m[2]), "s"(m[3])
               : "memory");
}
DEVI void ldpoll1(v4u& d, u64 m, rsrc_t r, u32 vd, u32 sd) {
  asm volatile("s_mov_b64 exec, %4\n\tbuffer_load_dwordx4 %0, %1, %2, %3 offen sc1\n\ts_mov_b64 exec, -1\n\ts_waitcnt vmcnt(0)"
               : "+v"(d) : "v"(vd), "s"(r), "s"(sd), "s"(m) : "memory");
}
DEVI u64 bal(bool c) { return __builtin_amdgcn_ballot_w64(c); }
DEVI float mx(float a, float b) { return __builtin_elementwise_maximum(a, b); }
#define SB() __builtin_amdgcn_sched_barrier(0)
#define PIN(x) asm volatile("" : "+v"(x))
#define VMW(n) asm volatile("s_waitcnt vmcnt(%0)" ::"n"(n) : "memory")

#define STAMPA(i)
#define STAMPB(i)
#define STAMP(i)
#define CNT(x)

DEVI v4u tagged(v4f v, u32 tag) {
  return (v4u){__builtin_amdgcn_perm(fu(v.x), tag, 0x07060500u), __builtin_amdgcn_perm(fu(v.y), tag, 0x07060501u),
               __builtin_amdgcn_perm(fu(v.z), tag, 0x07060502u), __builtin_amdgcn_perm(fu(v.w), tag, 0x07060503u)};
}
DEVI u32 tagof(v4u d) {
  return __builtin_amdgcn_perm(__builtin_amdgcn_perm(d.w, d.z, 0x0c0c0400u), __builtin_amdgcn_perm(d.y, d.x, 0x0c0c0400u), 0x05040100u);
}
DEVI v4f untag(v4u d) { return (v4f){uf(d.x & ~255u), uf(d.y & ~255u), uf(d.z & ~255u), uf(d.w & ~255u)}; }
DEVI u32 sum4(v4u d) { return d.x + d.y + d.z + d.w; }
template <int T> DEVI v4u onehot(u32 v, const u32* msk) {
  return (v4u){dppu<0x150 + T>(v) & msk[0], dppu<0x150 + T>(v) & msk[1], dppu<0x150 + T>(v) & msk[2], dppu<0x150 + T>(v) & msk[3]};
}
template <int I, int N, class F> DEVI void sfor(F&& f) {
  if constexpr (I < N) { f(std::integral_constant<int, I>{}); sfor<I + 1, N>(f); }
}
DEVI void lnstat(v4u xa, u32 xb, float& mb, float& rs) {
  const float pv = uf(__builtin_amdgcn_readfirstlane(xa.x) << 16);
  float xv[10] = {bflo(xa.x), bfhi(xa.x), bflo(xa.y), bfhi(xa.y), bflo(xa.z), bfhi(xa.z), bflo(xa.w), bfhi(xa.w), bflo(xb), bfhi(xb)};
  float s1a = 0.f, s1b = 0.f, s2a = 0.f, s2b = 0.f;
#pragma unroll
  for (int i = 0; i < 5; ++i) { float d = xv[2 * i] - pv, e = xv[2 * i + 1] - pv; s1a += d; s1b += e; s2a += d * d; s2b += e * e; }
  float s = sw32add(s1a + s1b, s2a + s2b);
  s = sw16add(s, s);
  s = rowsum(s);
  const float m1 = uf(__builtin_amdgcn_readlane(fu(s), 0)) * (1.0f / 640.0f), m2 = uf(__builtin_amdgcn_readlane(fu(s), 32)) * (1.0f / 640.0f);
  const float var = fmaxf(m2 - m1 * m1, 0.f);
  mb = rb(pv + m1);
  rs = __builtin_amdgcn_rcpf(rb(__builtin_amdgcn_sqrtf(rb(rb(var) + 1e-5f))));
}

__global__ void __launch_bounds__(512, 2)
mla_decode_kernel(const void* __restrict__ px, const void* __restrict__ pckv, const void* __restrict__ pwq, const void* __restrict__ pwuk,
                  void* __restrict__ pws, const void* __restrict__ pwuv, const void* __restrict__ pwo, const void* __restrict__ pw1,
                  const void* __restrict__ pw2, void* __restrict__ pout) {
  __shared__ __attribute__((aligned(1024))) char smem[L_END];
  u64 pc;
  asm volatile("s_getpc_b64 %0" : "=s"(pc));
  asm volatile("" ::"v"((LAS char*)smem));
  STAMP(0);
  const u32 tid = threadIdx.x, lane = tid & 63, n = lane & 15, g = lane >> 4;
  const u32 w = __builtin_amdgcn_readfirstlane(tid >> 6);
  const u32 b = blockIdx.x, xcd = b & 7, r = b >> 3;
  if (CODE_BYTES && w == 7) dma16(mk((const void*)(pc & ~255ull), CODE_BYTES), lane * 16, r * 1024, r < 16 ? L_X + 2048 : L_PFD + 7 * 1024);
  asm volatile("" ::"s"(px), "s"(pckv), "s"(pwq), "s"(pwuk), "s"(pwuv), "s"(pwo), "s"(pw1), "s"(pw2), "s"(pout), "s"(pws));
  const rsrc_t rws = mk(pws, WS_BYTES);
  const rsrc_t rx = mk(px, 1280);
  u64 ep;
  auto ep_load = [&] { asm volatile("s_load_dwordx2 %0, %1, 0x0" : "=s"(ep) : "s"(pws) : "memory"); };
  u32 tlo, thi, tgh;
  auto ep_use = [&] {
    asm volatile("" : "+s"(ep));
    tlo = (u32)(ep + 1); thi = (u32)((ep + 1) >> 32); tgh = tlo * 0x9e3779b1u + thi * 0x85ebca6bu;
  };

  if (__builtin_expect(r < 16, 1)) {
    const u32 h = xcd * 4 + (r & 3), cq = r >> 2, wi = h * 4 + cq;
    const rsrc_t rwq = mk(pwq, 640u * 4096 * 2), rwuk = mk(pwuk, 512u * 4096 * 2);
    if (w == 0) { dma16(rx, lane * 16, 0, L_X); dma16(rx, lane * 16, 1024, L_X + 1024); }
    if (w == 1) __builtin_amdgcn_raw_buffer_store_b32(~0u, rws, lane * 4, WS_QA + cq * 8192 + h * 256, 16);
    SB();
    v4u wq[20], wuk[4];
    const u32 wq_v = (4 * w + g) * 8192 + h * 256 + n * 16, wuk_v = g * 8192 + ((n ^ g) << 4);
    auto fld = [&](auto ic) {
      constexpr int i = ic;
      if constexpr (i >= UP && i < UP + 4) {
        dma16(rwuk, wuk_v ^ ((i - UP) << 6), (cq * 128 + 16 * w + 4 * (i - UP)) * 8192 + h * 256, L_UK + w * 4096 + (i - UP) * 1024);
      } else {
        constexpr int t = i < UP ? i : i - 4;
        u32 tt = t + 5 * cq;
        tt = tt >= 20 ? tt - 20 : tt;
        wq[t] = ld16p(rwq, wq_v, tt * 262144);
      }
    };
    sfor<0, WD>(fld);
    SB();
    STAMP(1);
    ep_load();
    u32 msk[4];
#pragma unroll
    for (int q = 0; q < 4; ++q) { msk[q] = (q == (int)((n & 7) >> 1)) ? ((n & 1) ? 0xffff0000u : 0x0000ffffu) : 0u; PIN(msk[q]); }
    u32 a_qw = L_QP + w * 320 + n * 16 + g * 8, a_qr = L_QP + (n & 7) * 320 + g * 16, a_pub = WS_QA + cq * 8192 + h * 256 + (((2 * w + (n >> 3)) ^ sig(h & 15)) << 4) + (n & 7) * 2;
    u32 t0 = n + 5 * cq, t1 = 16 + (n & 3) + 5 * cq;
    t0 = t0 >= 20 ? t0 - 20 : t0; t1 = t1 >= 20 ? t1 - 20 : t1;
    u32 a_x = L_X + lane * 16, a_xb = L_X + 1024 + lane * 4, a_x0 = L_X + (32 * t0 + 4 * w + g) * 2, a_x1 = L_X + (32 * t1 + 4 * w + g) * 2;
    u32 a_uk = L_UK + w * 4096 + n * 256 + ((g ^ n) << 4);
    PIN(a_qw); PIN(a_qr); PIN(a_pub); PIN(a_x); PIN(a_xb); PIN(a_x0); PIN(a_x1); PIN(a_uk);
    SB();
    VMW(WD);
    bar();
    ep_use();
    if (w == 1 && lane == 0) st8((v2u){tlo, thi}, rws, 0, WS_FL + wi * 8);
    u32 x0, x1;
    {
      const v4u xa = lds_ld<v4u>(a_x);
      const u32 xb = lds_ld<u32>(a_xb), xg0 = lds_ld<unsigned short>(a_x0), xg1 = lds_ld<unsigned short>(a_x1);
      SB();
      float mb, rs;
      lnstat(xa, xb, mb, rs);
      const u32 dp = pk(bflo(xg0) - mb, bflo(xg1) - mb);
      const float a0 = bflo(dp) * rs, a1 = bfhi(dp) * rs;
      x0 = pk(a0, a0); x1 = pk(a1, a1);
    }
    PIN(x0); PIN(x1);
    SB();
    STAMP(4);
    v4f qa = {0.f, 0.f, 0.f, 0.f};
    sfor<0, 24>([&](auto ic) {
      constexpr int i = ic, t = i < UP ? i : i - 4;
      constexpr bool isq = i < UP || i >= UP + 4;
      v4u oh;
      if constexpr (isq) { oh = onehot<(t & 15)>(t < 16 ? x0 : x1, msk); PIN(oh); }
      VMW(i + WD <= 24 ? WD - 1 : 23 - i);
      if constexpr (i + WD < 24) fld(std::integral_constant<int, i + WD>{});
      if constexpr (isq) { PIN(wq[t]); qa = mfma(oh, wq[t], qa); }
      if constexpr (i == UP + 3) {
#pragma unroll
        for (int u = 0; u < 4; ++u) wuk[u] = lds_ld<v4u>(a_uk ^ (u << 6));
      }
      if constexpr (i == 0) STAMP(3);
      if constexpr (i + WD == 23) STAMP(5);
      SB();
    });
    if (g < 2) lds_st<v2u>(a_qw, (v2u){pk(qa.x, qa.y), pk(qa.z, qa.w)});
    STAMP(6);
    bar();
    {
      v4u qf[4];
#pragma unroll
      for (int u = 0; u < 4; ++u) qf[u] = lds_ld<v4u>(a_qr + u * 64);
      v4f qb = {0.f, 0.f, 0.f, 0.f};
#pragma unroll
      for (int u = 0; u < 4; ++u) qb = mfma(qf[u], wuk[u], qb);
      float v = (qb.x + qb.y) + (qb.z + qb.w);
      v = sw16add(v, v) * (0.08838834764831845f * 1.4426950408889634f);
      const u32 p = pk(v, dpp<0xb1>(v));
      if ((lane & 0x31) == 0) __builtin_amdgcn_raw_buffer_store_b32(min(p, ~1u), rws, a_pub, 0, 16);
    }
    SB();
    STAMP(7);
    const rsrc_t rwuv = mk(pwuv, 512u * 4096 * 2), rwo = mk(pwo, 4096u * 640 * 2);
    const u32 uv_v = g * 8192 + ((n ^ ((((w & 3) << 1) | (g & 1)) << 1)) << 4), wo_v = g * 1280 + ((n ^ (((w & 1) * 4 + g) << 1)) << 4);
    const u32 q4 = n >> 2, p4 = n & 3;
    u32 a_wo[5], a_uv = L_UV + (16 * q4 + g) * 256 + ((w ^ ((q4 << 1) | (g & 1))) << 5) + p4 * 8;
#pragma unroll
    for (int k = 0; k < 5; ++k) {
      const u32 rho = 5 * w + (k < 4 ? p4 : 4), i = k < 4 ? k : p4;
      const u32 c4 = (rho >> 1) * 8 + 2 * i + (rho & 1);
      a_wo[k] = L_WO + (g * 5 + (c4 >> 5)) * 1024 + q4 * 256 + ((((c4 >> 2) & 7) ^ ((g & 1) * 4 + q4)) << 5) + (c4 & 3) * 8;
      PIN(a_wo[k]);
    }
    u32 a_cw = L_CP + w * 320 + n * 16 + g * 8, a_cr = L_CP + (n & 7) * 320 + g * 16, a_mr = L_ML + (g & 1) * 16;
    u32 a_xw = L_CX + ((((w >> 1) - cq) & 3) * 32 + (n >> 2) * 8 + (w & 1) * 4 + (n & 3)) * 2, a_xr = L_CX + g * 16;
    u32 a_in = g * OSK + n * 16, a_so = (lane >> 2) * 16384 + (lane & 3) * 16, a_ml = (4 * (n & 3) + g) * 512;
    u32 a_cs = (4 * (n & 3) + g) * 2048 + (n >> 3) * 256 + (((h >> 2) & 3) * 16 + (h & 1) * 8 + (n & 4) + (((h >> 4) ^ (n >> 3)) & 1) * 2 + ((h >> 1) & 1)) * 4;
    PIN(a_uv); PIN(a_cw); PIN(a_cr); PIN(a_mr); PIN(a_xw); PIN(a_xr); PIN(a_in); PIN(a_so); PIN(a_ml); PIN(a_cs);
    v4u uv[4], wo[4][5];
    SB();
    if (BK_SLEEP) __builtin_amdgcn_s_sleep(BK_SLEEP);
    static_assert(9 + BD <= 19 && UVP % 5 == 0 && UVP >= 5 && UVP <= 15, "quarter 3 reuses the slot of quarter 0");
    sfor<0, 24 + BD>([&](auto ic) {
      constexpr int i = ic;
      if constexpr (i >= BD) VMW(i < 24 ? BD - 1 : 23 - (i - BD));
      if constexpr (i >= 24) {
      } else if constexpr (i >= UVP && i < UVP + 4) {
        dma16(rwuv, uv_v, (128 * (w & 3) + 32 * cq + 16 * (w >> 2) + 4 * (i - UVP)) * 8192 + h * 256, L_UV + (4 * w + i - UVP) * 1024);
      } else {
        constexpr int j = i < UVP ? i : i - 4, u = j / 5, mb = j % 5;
        dma16(rwo, wo_v, (h * 128 + ((u + cq) & 3) * 32 + 4 * w) * 1280 + mb * 256, L_WO + (((u % 3) * 8 + w) * 5 + mb) * 1024);
      }
      constexpr int d = i - BD, dj = d < UVP ? d : d - 4;
      if constexpr (i == 0) STAMPA(14);
      if constexpr (i == BD - 1) STAMPA(2);
      if constexpr (d == UVP + 3) {
        STAMPA(13);
        bar();
#pragma unroll
        for (int u = 0; u < 4; ++u) uv[u] = trd2(a_uv + (64 * (u >> 1) + 4 * (u & 1)) * 256, a_uv + (64 * (u >> 1) + 4 * (u & 1) + 8) * 256);
        SB();
      } else if constexpr (d >= 0 && !(d >= UVP && d < UVP + 4) && dj % 5 == 4) {
        constexpr int u = dj / 5;
        if constexpr (u < 3) STAMPA(10 + u);
        bar();
        if constexpr (u == 3) STAMP(8);
#pragma unroll
        for (int k = 0; k < 5; ++k) wo[u][k] = trd2(a_wo[k] + (u % 3) * 40960, a_wo[k] + (u % 3) * 40960 + 20480);
        SB();
      }
    });
#pragma unroll
    for (int u = 0; u < 4; ++u) {
      PIN(uv[u]);
#pragma unroll
      for (int k = 0; k < 5; ++k) PIN(wo[u][k]);
    }
    SB();
    STAMP(9);
    const u32 s_ml = WS_ML + w * 8192 + h * 16, s_cs = WS_CS + w * 32768 + cq * 512, s_in = WS_IN3 + h * OSH + cq * OSC + 16 * w * OSK;
    const u32 s_i4[4] = {s_in, s_in + 4 * OSK, s_in + 8 * OSK, s_in + 12 * OSK};
    v4u ml, pv[4], af[4];
    asm volatile("" : "=v"(ml));
    {
      u64 bad = 0x000f000f000f000full;
      u32 sp = 0;
      _Pragma("nounroll") do {
        if (ML_SLEEP) __builtin_amdgcn_s_sleep(ML_SLEEP);
        ldpoll1(ml, bad, rws, a_ml, s_ml);
        bad &= bal((ml.z != tlo) | (ml.w != thi));
      } while (bad && ++sp < SPIN_CAP);
    }
    STAMPB(2);
    if (P_SLEEP) __builtin_amdgcn_s_sleep(P_SLEEP);
#pragma unroll
    for (int i = 0; i < 4; ++i) pv[i] = ld16a(rws, a_in, s_i4[i]);
    u32 cv = ld4a(rws, a_cs, s_cs);
    SB();
    {
      float mw = mx(uf(ml.x), dpp<0xb1>(uf(ml.x)));
      mw = mx(mw, dpp<0x4e>(mw));
      { auto q = __builtin_amdgcn_permlane16_swap(fu(mw), fu(mw), false, false); mw = mx(uf(q[0]), uf(q[1])); }
      { auto q = __builtin_amdgcn_permlane32_swap(fu(mw), fu(mw), false, false); mw = mx(uf(q[0]), uf(q[1])); }
      const float e = __builtin_amdgcn_exp2f(uf(ml.x) - mw);
      const u32 ee = pk(e, e);
      float lw = bfhi(ee) * uf(ml.y);
      lw += dpp<0xb1>(lw); lw += dpp<0x4e>(lw);
      lw = sw16add(lw, lw); lw = sw32add(lw, lw);
      if (lane < 2) lds_st<float>(L_ML + w * 4 + lane * 32, lane ? lw : mw);
      af[0] = onehot<0>(ee, msk); af[1] = onehot<1>(ee, msk); af[2] = onehot<2>(ee, msk); af[3] = onehot<3>(ee, msk);
#pragma unroll
      for (int i = 0; i < 4; ++i) PIN(af[i]);
    }
    SB();
    v4f c;
    u64 bad;
    auto ochk = [&](auto first) {
      u32 t[4];
      c = (v4f){0.f, 0.f, 0.f, 0.f};
      sfor<0, 4>([&](auto ic) {
        constexpr int i = ic;
        if constexpr (first) VMW(4 - i);
        PIN(pv[i]);
        c = mfma(af[i], pv[i], c);
        t[i] = sum4(pv[i]);
        t[i] += dppu<0xb1>(t[i]);
        if constexpr (i & 1) { t[i] = (n & 1) ? t[i] : t[i - 1]; t[i] += dppu<0x4e>(t[i]); }
        if constexpr (first) SB();
      });
      if constexpr (first) VMW(0);
      PIN(cv);
      bad = bal(((n & 2) ? t[3] : t[1]) + tgh != cv);
    };
    ochk(std::true_type{});
    if (__builtin_expect(bad != 0, 0)) {
      u32 sp = 0;
      _Pragma("nounroll") do {
        u64 bo[4];
#pragma unroll
        for (int i = 0; i < 4; ++i) bo[i] = ((bad >> i) & 0x1111111111111111ull) * 15;
        ldpoll(pv, cv, bo, bad, rws, a_in, s_i4, a_cs, s_cs);
        ochk(std::false_type{});
        CNT(nsp3);
      } while (bad && ++sp < SPIN_CAP);
    }
    STAMPB(10);
    if (g < 2) lds_st<v2u>(a_cw, (v2u){pk(c.x, c.y), pk(c.z, c.w)});
    STAMPB(11);
    bar();
    {
      v4u cf[4];
#pragma unroll
      for (int u = 0; u < 4; ++u) cf[u] = lds_ld<v4u>(a_cr + u * 64);
      const v4f m4 = lds_ld<v4f>(a_mr), l4 = lds_ld<v4f>(a_mr + 32);
      v4f cx = {0.f, 0.f, 0.f, 0.f};
#pragma unroll
      for (int u = 0; u < 4; ++u) cx = mfma(cf[u], uv[u], cx);
      const float mg = sw16max(fmaxf(fmaxf(m4.x, m4.y), fmaxf(m4.z, m4.w)));
      const float e0 = __builtin_amdgcn_exp2f(m4.x - mg), e1 = __builtin_amdgcn_exp2f(m4.y - mg);
      const float e2 = __builtin_amdgcn_exp2f(m4.z - mg), e3 = __builtin_amdgcn_exp2f(m4.w - mg);
      float den = (e0 * l4.x + e1 * l4.y) + (e2 * l4.z + e3 * l4.w);
      const float inv = __builtin_amdgcn_rcpf(sw16add(den, den));
      float v = (e0 * cx.x + e1 * cx.y) + (e2 * cx.z + e3 * cx.w);
      v = sw16add(v, v) * inv;
      if (g == 0) lds_st<unsigned short>(a_xw, (unsigned short)pk(v, v));
    }
    STAMPB(12);
    bar();
    {
      v4u xf[4];
#pragma unroll
      for (int u = 0; u < 4; ++u) xf[u] = lds_ld<v4u>(a_xr + u * 64);
      v4f s[5];
#pragma unroll
      for (int k = 0; k < 5; ++k) s[k] = (v4f){0.f, 0.f, 0.f, 0.f};
#pragma unroll
      for (int u = 0; u < 4; ++u)
#pragma unroll
        for (int k = 0; k < 5; ++k) s[k] = mfma(xf[u], wo[u][k], s[k]);
      const u32 s4 = fu(s[4].x);
      const v4f d = {uf(__builtin_amdgcn_update_dpp(fu(s[0].x), s4, 0xe4, 0x2, 0xf, false)), uf(__builtin_amdgcn_update_dpp(fu(s[1].x), s4, 0x104, 0x2, 0xf, false)),
                     uf(__builtin_amdgcn_update_dpp(fu(s[2].x), s4, 0x108, 0x2, 0xf, false)), uf(__builtin_amdgcn_update_dpp(fu(s[3].x), s4, 0x10c, 0x2, 0xf, false))};
      if (lane < 20) st16(tagged(d, tlo), rws, a_so, WS_IN5 + (5 * w * 256 + wi) * 64);
    }
    STAMPB(13);
  } else {
    const u32 ks = KSW & 1 ? xcd * 16 + r - 16 : b - 128, kc = KSW & 2 ? xcd * 16 + r - 16 : b - 128;
    const rsrc_t rck = mk(pckv, 16384u * 1024);
    u32 a_o = (4 * g + (n >> 3)) * OSH + (n & 7) * 16;
    const u32 s_o = WS_IN3 + (w >> 1) * OSC + ks * OSK + (w & 1) * 128;
    if (w == 0) { dma16(rx, lane * 16, 0, L_XK); dma16(rx, lane * 16, 1024, L_XK + 1024); }
    const u32 fb = xcd + 8 * ((r >> 2) & 3), yi = fb * 4 + (r & 3);
    float xs[4];
    const rsrc_t rwuk = mk(pwuk, 512u * 4096 * 2);
    const u32 ck_v = g * 1024 + ((n ^ (g << 2)) << 4), ck_s = (kc * 128 + 16 * w) * 1024;
    u32 ra[4], rq[4], va[2], a_pr[4], a_mx, a_pw, a_f1, a_f2;
    v4u kf[16], vt[4][4];
    auto stage = [&](auto sc) {
      constexpr int s = sc;
      bar();
#pragma unroll
      for (int u = 0; u < 4; ++u) kf[4 * s + u] = lds_ld<v4u>(L_CKV + ra[u] + w * 4096 + s * 32768);
#pragma unroll
      for (int kk = 0; kk < 4; ++kk) vt[kk][s] = trd2(va[0] + s * 32768 + kk * 8192, va[1] + s * 32768 + kk * 8192);
      if constexpr (s == 0) {
        ep_use();
        const v4u xa = lds_ld<v4u>(L_XK + lane * 16);
        const u32 xb = lds_ld<u32>(L_XK + 1024 + lane * 4);
        float mb, rs;
        lnstat(xa, xb, mb, rs);
#pragma unroll
        for (int i = 0; i < 4; ++i) {
          xs[i] = rb(rb(bflo(lds_ld<unsigned short>(L_XK + w * 256 + i * 64 + (lane & 62))) - mb) * rs);
          PIN(xs[i]);
        }
      }
      SB();
    };
    auto ckld = [&](int s, int j) { dma16s(rck, ck_v ^ (j << 4), ck_s + j * 4096 + s * 256, L_CKV + s * 32768 + (16 * w + 4 * j) * 256); };
    static_assert(KD >= 5 && KD <= 8 && QW < KD, "");
    constexpr int NA = PF + 17, IF = PF + FPOS;
    static_assert(IF < NA - QW && FPOS > 8, "");
    v2u fv;
    sfor<0, NA>([&](auto ic) {
      constexpr int i = ic;
      if constexpr (i >= KD) VMW(KD - 1);
      if constexpr (i == PF && KS) __builtin_amdgcn_s_sleep(KS);
      if constexpr (i < PF) dma16(rwuk, lane * 16, ((r - 16) * 32 + 4 * w + i) * 8192 + xcd * 1024, L_PFD + w * 1024);
      else if constexpr (i == IF) fv = ld8a(rws, n * 8, WS_FL + w * 128);
      else { constexpr int k = i - PF - (i > IF); ckld(k >> 2, k & 3); }
      if constexpr (i == KD - 1) {
        SB();
        STAMP(1);
        ep_load();
        const u32 q = n >> 2, p = n & 3;
#pragma unroll
        for (int u = 0; u < 4; ++u) { ra[u] = n * 256 + (((g * 4 + u) ^ sig(n)) << 4); PIN(ra[u]); }
#pragma unroll
        for (int t = 0; t < 2; ++t) {
          const u32 row = 8 * g + 4 * t + q;
          va[t] = L_CKV + row * 256 + (((2 * w + (p >> 1)) ^ sig(row & 15)) << 4) + 8 * (p & 1);
          PIN(va[t]);
        }
        a_mx = L_MX + n * 4; a_pw = L_P + n * 256 + (((2 * w + (g >> 1)) ^ n) << 4) + (g & 1) * 8;
#pragma unroll
        for (int kk = 0; kk < 4; ++kk) { a_pr[kk] = L_P + (w & 1) * 4096 + n * 256 + (((4 * kk + g) ^ n) << 4); PIN(a_pr[kk]); }
#pragma unroll
        for (int u = 0; u < 4; ++u) { rq[u] = ra[u] + L_QA; PIN(rq[u]); }
        a_f1 = (lane >> 1) * 3456 + (lane & 1) * 16; a_f2 = w < 4 ? (lane >> 3) * 3456 + (lane & 7) * 16 : lane * 16;
        PIN(a_mx); PIN(a_pw); PIN(a_o); PIN(a_f1); PIN(a_f2);
        SB();
      }
      if constexpr (i - KD == PF + 3) stage(std::integral_constant<int, 0>{});
      if constexpr (i - KD == PF + 7) stage(std::integral_constant<int, 1>{});
    });
    const rsrc_t rf = mk(w < 4 ? pw1 : pw2, fb < 27 && (FFP == 0 || w >= 4) ? 640u * 1728 * 2 : 0u);
    const u32 ff_s = w < 4 ? (160 * (r & 3) + 40 * w) * 3456 + fb * 128 : yi * 20480 + (w - 4) * 5120, ff_d = w < 4 ? 8 * 3456 : 1024;
    auto ffn_ld = [&](int i) { dma16(rf, a_f2, ff_s + i * ff_d, L_FF + w * 5120 + i * 1024); };
    if (w == 0 && lane < 32) lds_st<float>(L_MX + lane * 4, -__builtin_inff());
    u32 a_q = lane * 16, a_qs = L_QA + w * 1024 + lane * 16;
    u32 a_lw = L_L + ((g >> 1) * 16 + n) * 48 + w * 4, a_tl = L_L + ((w & 1) * 16 + n) * 48, a_ts = WS_CS + ks * 2048 + w * 256 + lane * 4;
    u32 a_ms = WS_ML + ks * 512 + (w & 1) * 256 + n * 16;
    PIN(a_q); PIN(a_qs); PIN(a_tl); PIN(a_ts); PIN(a_lw); PIN(a_ms);
    SB();
    if constexpr (QW < 4) { VMW(FPOS > 12 ? 5 : 4); stage(std::integral_constant<int, 2>{}); }
    VMW(QW);
    if (QS) __builtin_amdgcn_s_sleep(QS);
    v4u qr[4];
#pragma unroll
    for (int j = 0; j < 4; ++j) qr[j] = ld16a(rws, a_q, WS_QA + j * 8192 + w * 1024);
    STAMP(2);
    PIN(fv);
    u64 bf = bal((fv.x != tlo) | (fv.y != thi));
    if constexpr (QW >= 4) { VMW(8); stage(std::integral_constant<int, 2>{}); }
    if constexpr (S3B) { VMW(4); stage(std::integral_constant<int, 3>{}); }
    STAMP(3);
    u64 bq[4];
    auto qchk = [&](int j) {
      PIN(qr[j]);
      lds_st<v4u>(a_qs + j * 8192, qr[j]);
      bq[j] = bal(max(max(qr[j].x, qr[j].y), max(qr[j].z, qr[j].w)) == ~0u);
    };
    sfor<0, 4>([&](auto jc) { VMW(3 - jc); qchk(jc); SB(); });
    STAMP(4);
    if (__builtin_expect((bf | bq[0] | bq[1] | bq[2] | bq[3]) != 0, 0)) {
      const u32 s_q[4] = {WS_QA + w * 1024, WS_QA + 8192 + w * 1024, WS_QA + 16384 + w * 1024, WS_QA + 24576 + w * 1024};
      u32 sp = 0;
      if (bf) {
        _Pragma("nounroll") do {
          asm volatile("" ::: "memory");
          fv = __builtin_amdgcn_raw_buffer_load_b64(rws, n * 8, WS_FL + w * 128, 16);
        } while (bal((fv.x != tlo) | (fv.y != thi)) && ++sp < SPIN_CAP);
        bq[0] = bq[1] = bq[2] = bq[3] = ~0ull;
      }
      _Pragma("nounroll") do {
        if (QRS) __builtin_amdgcn_s_sleep(QRS);
        ldpoll4(qr, bq, rws, a_q, s_q);
#pragma unroll
        for (int j = 0; j < 4; ++j) qchk(j);
        CNT(nsp2);
      } while ((bq[0] | bq[1] | bq[2] | bq[3]) && ++sp < SPIN_CAP);
    }
    bar();
    STAMP(5);
    v4f sc[2] = {{0.f, 0.f, 0.f, 0.f}, {0.f, 0.f, 0.f, 0.f}};
    {
      v4u qf[32];
      auto qrd = [&](auto jc) { constexpr int j = jc, u = j >> 1; qf[j] = lds_ld<v4u>(rq[u & 3] + (j & 1) * 4096 + (u >> 2) * 8192); };
      sfor<0, SD>(qrd);
      SB();
      sfor<0, 32>([&](auto jc) {
        constexpr int j = jc;
        sc[j & 1] = mfma(kf[j >> 1], qf[j], sc[j & 1]);
        if constexpr (j + SD < 32) qrd(std::integral_constant<int, j + SD>{});
        if constexpr (FFP < 2 && j % FSP == 0 && j / FSP >= 1 && j / FSP <= 5) ffn_ld(j / FSP - 1);
        if constexpr (!S3B && j + SD == 32) {
#pragma unroll
          for (int u = 0; u < 4; ++u) kf[12 + u] = lds_ld<v4u>(L_CKV + ra[u] + w * 4096 + 3 * 32768);
        }
        SB();
      });
    }
    STAMP(6);
#pragma unroll
    for (int ht = 0; ht < 2; ++ht)
      __hip_atomic_fetch_max((LAS float*)(a_mx + ht * 64), mx(mx(sc[ht].x, sc[ht].y), mx(sc[ht].z, sc[ht].w)), __ATOMIC_RELAXED,
                             __HIP_MEMORY_SCOPE_WORKGROUP);
    STAMP(7);
    bar();
    float mrow[2], ls[2];
#pragma unroll
    for (int ht = 0; ht < 2; ++ht) mrow[ht] = lds_ld<float>(a_mx + ht * 64);
    if constexpr (!S3B) {
#pragma unroll
      for (int kk = 0; kk < 4; ++kk) vt[kk][3] = trd2(va[0] + 3 * 32768 + kk * 8192, va[1] + 3 * 32768 + kk * 8192);
    }
    SB();
#pragma unroll
    for (int ht = 0; ht < 2; ++ht) {
      const float p0 = __builtin_amdgcn_exp2f(sc[ht].x - mrow[ht]), p1 = __builtin_amdgcn_exp2f(sc[ht].y - mrow[ht]);
      const float p2 = __builtin_amdgcn_exp2f(sc[ht].z - mrow[ht]), p3 = __builtin_amdgcn_exp2f(sc[ht].w - mrow[ht]);
      lds_st<v2u>(a_pw + ht * 4096, (v2u){pk(p0, p1), pk(p2, p3)});
      ls[ht] = (p0 + p1) + (p2 + p3);
    }
    {
      const float x = sw32add(ls[0], ls[1]);
      lds_st<float>(a_lw, sw16add(x, x));
    }
    STAMP(8);
    if (w >= 4) __builtin_amdgcn_s_setprio(1);
    bar();
    v4u pf[2][4];
#pragma unroll
    for (int ht = 0; ht < 2; ++ht)
#pragma unroll
      for (int kk = 0; kk < 4; ++kk) pf[ht][kk] = lds_ld<v4u>(a_pr[kk] ^ (ht * 4096));
    u32 cs[2];
    auto mlst = [&] {
      if ((w >> 1) == MLW) {
        const v4f l4 = lds_ld<v4f>(a_tl) + lds_ld<v4f>(a_tl + 16);
        if (g == 0) st16((v4u){fu((w & 1) ? mrow[1] : mrow[0]), fu((l4.x + l4.y) + (l4.z + l4.w)), tlo, thi}, rws, a_ms, 0);
      }
    };
    if (MLP == 0) mlst();
    SB();
    STAMPA(10);
#pragma unroll
    for (int ht = 0; ht < 2; ++ht) {
      v4f o[4];
#pragma unroll
      for (int ct = 0; ct < 4; ++ct) o[ct] = (v4f){0.f, 0.f, 0.f, 0.f};
#pragma unroll
      for (int kk = 0; kk < 4; ++kk)
#pragma unroll
        for (int ct = 0; ct < 4; ++ct) {
          o[ct] = mfma(pf[ht][kk], vt[kk][ct], o[ct]);
        }
      if (MLP == 1 + 2 * ht) mlst();
      STAMPA(11 + 2 * ht);
#pragma unroll
      for (int pr = 0; pr < 2; ++pr) {
        const u32 x0 = pk(o[0][2 * pr], o[1][2 * pr]), x1 = pk(o[2][2 * pr], o[3][2 * pr]);
        const u32 y0 = pk(o[0][2 * pr + 1], o[1][2 * pr + 1]), y1 = pk(o[2][2 * pr + 1], o[3][2 * pr + 1]);
        const v4u d = {(u32)__builtin_amdgcn_update_dpp(x0, y0, 0x128, 0xf, 0xc, false), (u32)__builtin_amdgcn_update_dpp(x1, y1, 0x128, 0xf, 0xc, false),
                       (u32)__builtin_amdgcn_update_dpp(y0, x0, 0x128, 0xf, 0x3, false), (u32)__builtin_amdgcn_update_dpp(y1, x1, 0x128, 0xf, 0x3, false)};
        st16(d, rws, a_o, s_o + (16 * (ht ^ (w & 1)) + 2 * pr) * OSH);
        u32 t = sum4(d);
        t += dppu<0xb1>(t);
        cs[ht] = pr && !(n & 1) ? cs[ht] : t;
      }
      cs[ht] += dppu<0x4e>(cs[ht]);
      if (MLP == 2 + 2 * ht) mlst();
      STAMPA(12 + 2 * ht);
      if (PVS) SB();
    }
    __builtin_amdgcn_raw_buffer_store_b32(((n & 2) ? cs[1] : cs[0]) + tgh, rws, a_ts, 0, 16);
    STAMP(9);
    if (fb < 27) {
      if (FBAR) __builtin_amdgcn_s_barrier();
      if (FFP == 2 && w >= 4) {
#pragma unroll
        for (int i = 0; i < 5; ++i) ffn_ld(i);
      }
      if (w >= 5) VMW(FFP == 2 ? 0 : 5);
      else {
        v4u w1r[4], w2r[4];
        const rsrc_t rw1 = mk(pw1, 640u * 1728 * 2);
#pragma unroll
        for (int i = 0; i < 4; ++i) w1r[i] = ld16p(rw1, a_f1, (128 * w + 32 * i) * 3456 + yi * 32);
        VMW(0);
#pragma unroll
        for (int i = 0; i < 4; ++i) PIN(w1r[i]);
        STAMPB(10);
        float ha[8];
#pragma unroll
        for (int e = 0; e < 8; ++e) ha[e] = 0.f;
#pragma unroll
        for (int i = 0; i < 4; ++i) {
#pragma unroll
          for (int q = 0; q < 4; ++q) { ha[2 * q] += xs[i] * bflo(w1r[i][q]); ha[2 * q + 1] += xs[i] * bfhi(w1r[i][q]); }
        }
        float h0 = sw16add(sw32add(ha[0], ha[2]), sw32add(ha[1], ha[3]));
        float h1 = sw16add(sw32add(ha[4], ha[6]), sw32add(ha[5], ha[7]));
        h0 += dpp<0x128>(h0); h1 += dpp<0x128>(h1); h0 += dpp<0x124>(h0); h1 += dpp<0x124>(h1); h0 += dpp<0x122>(h0); h1 += dpp<0x122>(h1);
        if (n < 4) lds_st<float>(L_QA + w * 64 + ((n & 1) * 8 + (n >> 1) * 4 + g) * 4, n < 2 ? h0 : h1);
        STAMPB(11);
        bar();
#pragma unroll
        for (int i = 0; i < 4; ++i) w2r[i] = lds_ld<v4u>(L_FF + 20480 + (4 * i + g) * 1280 + w * 256 + n * 16);
        float hs = 0.f;
#pragma unroll
        for (int v = 0; v < 5; ++v) hs += lds_ld<float>(L_QA + v * 64 + (4 * (n & 3) + g) * 4);
        hs = fmaxf(hs, 0.f);
        const u32 hh = pk(hs, hs);
        u32 msk[4];
#pragma unroll
        for (int q = 0; q < 4; ++q) msk[q] = (q == (int)((n & 7) >> 1)) ? ((n & 1) ? 0xffff0000u : 0x0000ffffu) : 0u;
        v4f y = mfma(onehot<0>(hh, msk), w2r[0], (v4f){0.f, 0.f, 0.f, 0.f});
        y = mfma(onehot<1>(hh, msk), w2r[1], y);
        y = mfma(onehot<2>(hh, msk), w2r[2], y);
        y = mfma(onehot<3>(hh, msk), w2r[3], y);
        if (g < 2) st16(tagged(y, tlo), rws, ((n >> 2) * 2 + g) * 16384 + (n & 3) * 16, WS_IN5 + (w * 8 * 256 + 128 + yi) * 64);
        STAMPB(12);
      }
    } else if (w < 2) {
      const u32 rho = (yi - 108) * 2 + w;
      const u32 kg = (g & 1) * 2 + (g >> 1);
      const u32 col = (rho >> 1) * 32 + 8 * kg + 4 * (rho & 1) + (n & 3);
      const u32 xo = __builtin_amdgcn_raw_buffer_load_b16(rx, col * 2, 0, 0);
      float gt;
      {
        v4u yv[7];
        __builtin_amdgcn_s_sleep(Y_SLEEP);
        _Pragma("nounroll") for (u32 sp = 0;; ++sp) {
          asm volatile("" ::: "memory");
#pragma unroll
          for (int i = 0; i < 7; ++i) yv[i] = ld16<16>(rws, (i == 6 && lane >= 48 ? lane - 48 : lane) * 16 + (i & 3) * 1024, WS_IN5 + rho * 16384 + 8192 + (i >> 2) * 4096);
          bool ok = true;
#pragma unroll
          for (int i = 0; i < 7; ++i) ok &= tagof(yv[i]) == tlo;
          if (__all(ok) || sp >= SPIN_CAP) break;
          __builtin_amdgcn_s_sleep(4);
        }
        v4f a = lane < 48 ? untag(yv[6]) : (v4f){0.f, 0.f, 0.f, 0.f};
#pragma unroll
        for (int i = 0; i < 6; ++i) a += untag(yv[i]);
        float y = sw16add(sw32add(a.x, a.y), sw32add(a.z, a.w));
        y += dpp<0x128>(y); y += dpp<0x124>(y);
        const u32 n3 = n & 3;
        y = uf(__builtin_amdgcn_ds_bpermute((int)(((((n3 & 1) * 2 + (n3 >> 1)) << 4) + kg) * 4), fu(y)));
        y = rb(y);
        gt = rb(y / (1.0f + __expf(-y)));
      }
      float xf = bflo(xo);
      PIN(gt); PIN(xf);
      STAMPB(13);
      __builtin_amdgcn_s_sleep(RED_SLEEP);
      v4u sv[8];
      _Pragma("nounroll") for (u32 sp = 0;; ++sp) {
        asm volatile("" ::: "memory");
#pragma unroll
        for (int i = 0; i < 8; ++i) sv[i] = ld16<16>(rws, lane * 16 + (i & 3) * 1024, WS_IN5 + rho * 16384 + (i >> 2) * 4096);
        bool ok = true;
#pragma unroll
        for (int i = 0; i < 8; ++i) ok &= tagof(sv[i]) == tlo;
        if (__all(ok) || sp >= SPIN_CAP) break;
        CNT(nsp5);
      }
      STAMPB(14);
      v4f a = untag(sv[0]);
#pragma unroll
      for (int i = 1; i < 8; ++i) a += untag(sv[i]);
      float s = sw16add(sw32add(a.x, a.y), sw32add(a.z, a.w));
      s += dpp<0x128>(s); s += dpp<0x124>(s);
      const u32 o = pk(xf + rb(rb(s) * gt), 0.f);
      if (n < 4) ((unsigned short*)pout)[col] = (unsigned short)o;
      if (rho == 0 && lane == 0) __builtin_amdgcn_raw_buffer_store_b64((v2u){tlo, thi}, rws, 0, 0, 16);
    } else {
      VMW(5);
    }
  }
}

extern "C" void kernel_launch(void* const* d_in, const int* in_sizes, int n_in, void* d_out, int out_size, void* d_ws, size_t ws_size,
                              hipStream_t stream) {
  mla_decode_kernel<<<256, 512, 0, stream>>>(d_in[0], d_in[1], d_in[2], d_in[3], d_ws, d_in[4], d_in[5], d_in[7], d_in[8], d_out);
}
```

```cpp
#define STAMPS
#define STAMPMASK 32769
#define WHICH 15
#define KMUL 0
#define CLSX 2
#define MEAN 0
#include <hip/hip_runtime.h>
#include <type_traits>

typedef unsigned u32;
typedef unsigned long long u64;
typedef u32 v4u __attribute__((ext_vector_type(4)));
typedef u32 v2u __attribute__((ext_vector_type(2)));
typedef float v4f __attribute__((ext_vector_type(4)));
typedef float v2f __attribute__((ext_vector_type(2)));
typedef __bf16 bfx8 __attribute__((ext_vector_type(8)));
typedef __bf16 bfx2 __attribute__((ext_vector_type(2)));
typedef short v4s __attribute__((ext_vector_type(4)));
typedef __amdgpu_buffer_rsrc_t rsrc_t;
#define LAS __attribute__((address_space(3)))
#define DEVI __device__ __forceinline__

#define WD 6
#define UP 10
#define FFP 0
#define UVP 5
#define BD 8
#define CODE_BYTES 16384
#define KD 8
#define S3B 1
#define QW 2
#define FSP 4
#define KSW 1
#define FPOS 12
#define QRS 0
#define OSH 1024
#define OSC 256
#define OSK 32768
#define MLP 1
#define MLW 0
#define QS 0
#define PVS 1
#define FBAR 1
#define KS 8
#define SD 12
#define PF 4
#define BK_SLEEP 16
#define RED_SLEEP 0
#define Y_SLEEP 8
#define ML_SLEEP 4
#define P_SLEEP 0
#define AUX_CKV " nt"

constexpr u32 WS_FL = 2048, WS_QA = 4096, WS_ML = 131072, WS_CS = 262144, WS_IN3 = 1u << 20, WS_IN5 = 6u << 20, WS_BYTES = 8u << 20;
constexpr u32 WS_DBG = 16u << 20;
constexpr u32 L_END = 163840;
constexpr u32 L_CKV = 0, L_QA = 131072, L_MX = 0, L_L = 256, L_P = 8192, L_FF = 16384, L_XK = L_QA, L_PFD = L_QA + 8192;
constexpr u32 L_X = 0, L_UK = 4096, L_WO = 0, L_UV = 122880, L_QP = 155648, L_CP = L_UV, L_ML = L_UV + 4096, L_CX = L_UV + 4352;
constexpr u32 SPIN_CAP = 100000;

DEVI rsrc_t mk(const void* p, u32 bytes) { return __builtin_amdgcn_make_buffer_rsrc(const_cast<void*>(p), 0, bytes, 0x00020000); }
template <int AUX> DEVI v4u ld16(rsrc_t r, u32 v, u32 s) { return __builtin_amdgcn_raw_buffer_load_b128(r, v, s, AUX); }
DEVI void st16(v4u d, rsrc_t r, u32 v, u32 s) {
  __builtin_amdgcn_raw_buffer_store_b128(d, r, v, s, 16);
  asm volatile("s_nop 1" ::"v"(d));
}
DEVI float bflo(u32 x) { return __builtin_bit_cast(float, x << 16); }
DEVI float bfhi(u32 x) { return __builtin_bit_cast(float, x & 0xffff0000u); }
DEVI u32 pk(float a, float b) { v2f t = {a, b}; return __builtin_bit_cast(u32, __builtin_convertvector(t, bfx2)); }
DEVI float rb(float a) { return bfhi(pk(a, a)); }
DEVI u32 fu(float a) { return __builtin_bit_cast(u32, a); }
DEVI float uf(u32 a) { return __builtin_bit_cast(float, a); }
template <int C> DEVI float dpp(float x) { return uf(__builtin_amdgcn_update_dpp(0, fu(x), C, 0xf, 0xf, true)); }
template <int C> DEVI u32 dppu(u32 x) { return __builtin_amdgcn_update_dpp(0, x, C, 0xf, 0xf, true); }
DEVI float rowsum(float x) { x += dpp<0x128>(x); x += dpp<0x124>(x); x += dpp<0x122>(x); x += dpp<0x121>(x); return x; }
DEVI float sw32add(float a, float b) { auto p = __builtin_amdgcn_permlane32_swap(fu(a), fu(b), false, false); return uf(p[0]) + uf(p[1]); }
DEVI float sw16add(float a, float b) { auto p = __builtin_amdgcn_permlane16_swap(fu(a), fu(b), false, false); return uf(p[0]) + uf(p[1]); }
DEVI float sw32max(float a) { auto p = __builtin_amdgcn_permlane32_swap(fu(a), fu(a), false, false); return fmaxf(uf(p[0]), uf(p[1])); }
DEVI float sw16max(float a) { auto p = __builtin_amdgcn_permlane16_swap(fu(a), fu(a), false, false); return fmaxf(uf(p[0]), uf(p[1])); }
DEVI v4f mfma(v4u a, v4u b, v4f c) { return __builtin_amdgcn_mfma_f32_16x16x32_bf16(__builtin_bit_cast(bfx8, a), __builtin_bit_cast(bfx8, b), c, 0, 0, 0); }
DEVI float dot2(u32 a, u32 b, float c) { return __builtin_amdgcn_fdot2_f32_bf16(__builtin_bit_cast(bfx2, a), __builtin_bit_cast(bfx2, b), c, false); }
DEVI u32 sig(u32 r) { return ((r & 3) << 2) | ((r >> 2) & 3); }
template <class T> DEVI T lds_ld(u32 a) { return *(LAS T*)a; }
template <class T> DEVI void lds_st(u32 a, T v) { *(LAS T*)a = v; }
DEVI v2u trd(u32 a) { return __builtin_bit_cast(v2u, __builtin_amdgcn_ds_read_tr16_b64_v4i16((LAS v4s*)a)); }
DEVI v4u trd2(u32 a, u32 b) { v2u x = trd(a), y = trd(b); return (v4u){x.x, x.y, y.x, y.y}; }
DEVI void bar() { asm volatile("s_waitcnt lgkmcnt(0)\n\ts_barrier" ::: "memory"); }
DEVI void dma16(rsrc_t r, u32 voff, u32 soff, u32 ldsdst) {
  asm volatile("s_mov_b32 m0, %3\n\ts_nop 0\n\tbuffer_load_dwordx4 %0, %1, %2 offen lds" ::"v"(voff), "s"(r), "s"(soff), "s"(ldsdst) : "memory");
}
DEVI void dma16s(rsrc_t r, u32 voff, u32 soff, u32 ldsdst) {
  asm volatile("s_mov_b32 m0, %3\n\ts_nop 0\n\tbuffer_load_dwordx4 %0, %1, %2 offen" AUX_CKV " lds" ::"v"(voff), "s"(r), "s"(soff), "s"(ldsdst) : "memory");
}
DEVI void st8(v2u d, rsrc_t r, u32 v, u32 s) { __builtin_amdgcn_raw_buffer_store_b64(d, r, v, s, 16); }
DEVI v4u ld16a(rsrc_t r, u32 voff, u32 soff) {
  v4u d;
  asm volatile("buffer_load_dwordx4 %0, %1, %2, %3 offen sc1" : "=v"(d) : "v"(voff), "s"(r), "s"(soff) : "memory");
  return d;
}
DEVI v4u ld16p(rsrc_t r, u32 voff, u32 soff) {
  v4u d;
  asm volatile("buffer_load_dwordx4 %0, %1, %2, %3 offen" : "=v"(d) : "v"(voff), "s"(r), "s"(soff) : "memory");
  return d;
}
DEVI v2u ld8a(rsrc_t r, u32 voff, u32 soff) {
  v2u d;
  asm volatile("buffer_load_dwordx2 %0, %1, %2, %3 offen sc1" : "=v"(d) : "v"(voff), "s"(r), "s"(soff) : "memory");
  return d;
}
DEVI u32 ld4a(rsrc_t r, u32 voff, u32 soff) {
  u32 d;
  asm volatile("buffer_load_dword %0, %1, %2, %3 offen sc1" : "=v"(d) : "v"(voff), "s"(r), "s"(soff) : "memory");
  return d;
}
DEVI void ldpoll(v4u* d, u32& t, const u64* m, u64 mt, rsrc_t r, u32 vd, const u32* sd, u32 vt, u32 st) {
  asm volatile("s_mov_b64 exec, %12\n\tbuffer_load_dwordx4 %0, %5, %7, %17 offen sc1\n\t"
               "s_mov_b64 exec, %13\n\tbuffer_load_dwordx4 %1, %5, %7, %9 offen sc1\n\t"
               "s_mov_b64 exec, %14\n\tbuffer_load_dwordx4 %2, %5, %7, %10 offen sc1\n\t"
               "s_mov_b64 exec, %15\n\tbuffer_load_dwordx4 %3, %5, %7, %11 offen sc1\n\t"
               "s_mov_b64 exec, %16\n\tbuffer_load_dword %4, %6, %7, %8 offen sc1\n\t"
               "s_mov_b64 exec, -1\n\ts_waitcnt vmcnt(0)"
               : "+v"(d[0]), "+v"(d[1]), "+v"(d[2]), "+v"(d[3]), "+v"(t)
               : "v"(vd), "v"(vt), "s"(r), "s"(st), "s"(sd[1]), "s"(sd[2]), "s"(sd[3]), "s"(m[0]), "s"(m[1]), "s"(m[2]), "s"(m[3]), "s"(mt), "s"(sd[0])
               : "memory");
}
DEVI void ldpoll4(v4u* d, const u64* m, rsrc_t r, u32 vd, const u32* sd) {
  asm volatile("s_mov_b64 exec, %10\n\tbuffer_load_dwordx4 %0, %4, %5, %6 offen sc1\n\t"
               "s_mov_b64 exec, %11\n\tbuffer_load_dwordx4 %1, %4, %5, %7 offen sc1\n\t"
               "s_mov_b64 exec, %12\n\tbuffer_load_dwordx4 %2, %4, %5, %8 offen sc1\n\t"
               "s_mov_b64 exec, %13\n\tbuffer_load_dwordx4 %3, %4, %5, %9 offen sc1\n\t"
               "s_mov_b64 exec, -1\n\ts_waitcnt vmcnt(0)"
               : "+v"(d[0]), "+v"(d[1]), "+v"(d[2]), "+v"(d[3])
               : "v"(vd), "s"(r), "s"(sd[0]), "s"(sd[1]), "s"(sd[2]), "s"(sd[3]), "s"(m[0]), "s"(m[1]), "s"(m[2]), "s"(m[3])
               : "memory");
}
DEVI void ldpoll1(v4u& d, u64 m, rsrc_t r, u32 vd, u32 sd) {
  asm volatile("s_mov_b64 exec, %4\n\tbuffer_load_dwordx4 %0, %1, %2, %3 offen sc1\n\ts_mov_b64 exec, -1\n\ts_waitcnt vmcnt(0)"
               : "+v"(d) : "v"(vd), "s"(r), "s"(sd), "s"(m) : "memory");
}
DEVI u64 bal(bool c) { return __builtin_amdgcn_ballot_w64(c); }
DEVI float mx(float a, float b) { return __builtin_elementwise_maximum(a, b); }
#define SB() __builtin_amdgcn_sched_barrier(0)
#define PIN(x) asm volatile("" : "+v"(x))
#define VMW(n) asm volatile("s_waitcnt vmcnt(%0)" ::"n"(n) : "memory")

#define STAMP(i) do { if (!((STAMPMASK >> (i)) & 1)) break; SB(); stamp[i] = (u32)__builtin_amdgcn_s_memrealtime(); asm volatile("s_waitcnt lgkmcnt(0)" ::: "memory"); SB(); } while (0)
#define CNT(x) ++x
#define STAMPA(i)
#define STAMPB(i) STAMP(i)

DEVI v4u tagged(v4f v, u32 tag) {
  return (v4u){__builtin_amdgcn_perm(fu(v.x), tag, 0x07060500u), __builtin_amdgcn_perm(fu(v.y), tag, 0x07060501u),
               __builtin_amdgcn_perm(fu(v.z), tag, 0x07060502u), __builtin_amdgcn_perm(fu(v.w), tag, 0x07060503u)};
}
DEVI u32 tagof(v4u d) {
  return __builtin_amdgcn_perm(__builtin_amdgcn_perm(d.w, d.z, 0x0c0c0400u), __builtin_amdgcn_perm(d.y, d.x, 0x0c0c0400u), 0x05040100u);
}
DEVI v4f untag(v4u d) { return (v4f){uf(d.x & ~255u), uf(d.y & ~255u), uf(d.z & ~255u), uf(d.w & ~255u)}; }
DEVI u32 sum4(v4u d) { return d.x + d.y + d.z + d.w; }
template <int T> DEVI v4u onehot(u32 v, const u32* msk) {
  return (v4u){dppu<0x150 + T>(v) & msk[0], dppu<0x150 + T>(v) & msk[1], dppu<0x150 + T>(v) & msk[2], dppu<0x150 + T>(v) & msk[3]};
}
template <int I, int N, class F> DEVI void sfor(F&& f) {
  if constexpr (I < N) { f(std::integral_constant<int, I>{}); sfor<I + 1, N>(f); }
}
DEVI void lnstat(v4u xa, u32 xb, float& mb, float& rs) {
  const float pv = uf(__builtin_amdgcn_readfirstlane(xa.x) << 16);
  float xv[10] = {bflo(xa.x), bfhi(xa.x), bflo(xa.y), bfhi(xa.y), bflo(xa.z), bfhi(xa.z), bflo(xa.w), bfhi(xa.w), bflo(xb), bfhi(xb)};
  float s1a = 0.f, s1b = 0.f, s2a = 0.f, s2b = 0.f;
#pragma unroll
  for (int i = 0; i < 5; ++i) { float d = xv[2 * i] - pv, e = xv[2 * i + 1] - pv; s1a += d; s1b += e; s2a += d * d; s2b += e * e; }
  float s = sw32add(s1a + s1b, s2a + s2b);
  s = sw16add(s, s);
  s = rowsum(s);
  const float m1 = uf(__builtin_amdgcn_readlane(fu(s), 0)) * (1.0f / 640.0f), m2 = uf(__builtin_amdgcn_readlane(fu(s), 32)) * (1.0f / 640.0f);
  const float var = fmaxf(m2 - m1 * m1, 0.f);
  mb = rb(pv + m1);
  rs = __builtin_amdgcn_rcpf(rb(__builtin_amdgcn_sqrtf(rb(rb(var) + 1e-5f))));
}

__global__ void __launch_bounds__(512, 2)
mla_decode_kernel(const void* __restrict__ px, const void* __restrict__ pckv, const void* __restrict__ pwq, const void* __restrict__ pwuk,
                  void* __restrict__ pws, const void* __restrict__ pwuv, const void* __restrict__ pwo, const void* __restrict__ pw1,
                  const void* __restrict__ pw2, void* __restrict__ pout) {
  __shared__ __attribute__((aligned(1024))) char smem[L_END];
  u64 pc;
  asm volatile("s_getpc_b64 %0" : "=s"(pc));
  asm volatile("" ::"v"((LAS char*)smem));
  u32 stamp[16];
#pragma unroll
  for (int i = 0; i < 16; ++i) stamp[i] = 0;
  u32 nsp2 = 0, nsp3 = 0, nsp5 = 0;
  const u32 clk0 = (u32)__builtin_amdgcn_s_memtime();
  STAMP(0);
  const u32 tid = threadIdx.x, lane = tid & 63, n = lane & 15, g = lane >> 4;
  const u32 w = __builtin_amdgcn_readfirstlane(tid >> 6);
  const u32 b = blockIdx.x, xcd = b & 7, r = b >> 3;
  if (CODE_BYTES && w == 7) dma16(mk((const void*)(pc & ~255ull), CODE_BYTES), lane * 16, r * 1024, r < 16 ? L_X + 2048 : L_PFD + 7 * 1024);
  asm volatile("" ::"s"(px), "s"(pckv), "s"(pwq), "s"(pwuk), "s"(pwuv), "s"(pwo), "s"(pw1), "s"(pw2), "s"(pout), "s"(pws));
  const rsrc_t rws = mk(pws, WS_BYTES);
  const rsrc_t rx = mk(px, 1280);
  u64 ep;
  auto ep_load = [&] { asm volatile("s_load_dwordx2 %0, %1, 0x0" : "=s"(ep) : "s"(pws) : "memory"); };
  u32 tlo, thi, tgh;
  auto ep_use = [&] {
    asm volatile("" : "+s"(ep));
    tlo = (u32)(ep + 1); thi = (u32)((ep + 1) >> 32); tgh = tlo * 0x9e3779b1u + thi * 0x85ebca6bu;
  };

  if (__builtin_expect(r < 16, 1)) {
    const u32 h = xcd * 4 + (r & 3), cq = r >> 2, wi = h * 4 + cq;
    const rsrc_t rwq = mk(pwq, 640u * 4096 * 2), rwuk = mk(pwuk, 512u * 4096 * 2);
    if (w == 0) { dma16(rx, lane * 16, 0, L_X); dma16(rx, lane * 16, 1024, L_X + 1024); }
    if (w == 1) __builtin_amdgcn_raw_buffer_store_b32(~0u, rws, lane * 4, WS_QA + cq * 8192 + h * 256, 16);
    SB();
    v4u wq[20], wuk[4];
    const u32 wq_v = (4 * w + g) * 8192 + h * 256 + n * 16, wuk_v = g * 8192 + ((n ^ g) << 4);
    auto fld = [&](auto ic) {
      constexpr int i = ic;
      if constexpr (i >= UP && i < UP + 4) {
        dma16(rwuk, wuk_v ^ ((i - UP) << 6), (cq * 128 + 16 * w + 4 * (i - UP)) * 8192 + h * 256, L_UK + w * 4096 + (i - UP) * 1024);
      } else {
        constexpr int t = i < UP ? i : i - 4;
        u32 tt = t + 5 * cq;
        tt = tt >= 20 ? tt - 20 : tt;
        wq[t] = ld16p(rwq, wq_v, tt * 262144);
      }
    };
    sfor<0, WD>(fld);
    SB();
    STAMP(1);
    ep_load();
    u32 msk[4];
#pragma unroll
    for (int q = 0; q < 4; ++q) { msk[q] = (q == (int)((n & 7) >> 1)) ? ((n & 1) ? 0xffff0000u : 0x0000ffffu) : 0u; PIN(msk[q]); }
    u32 a_qw = L_QP + w * 320 + n * 16 + g * 8, a_qr = L_QP + (n & 7) * 320 + g * 16, a_pub = WS_QA + cq * 8192 + h * 256 + (((2 * w + (n >> 3)) ^ sig(h & 15)) << 4) + (n & 7) * 2;
    u32 t0 = n + 5 * cq, t1 = 16 + (n & 3) + 5 * cq;
    t0 = t0 >= 20 ? t0 - 20 : t0; t1 = t1 >= 20 ? t1 - 20 : t1;
    u32 a_x = L_X + lane * 16, a_xb = L_X + 1024 + lane * 4, a_x0 = L_X + (32 * t0 + 4 * w + g) * 2, a_x1 = L_X + (32 * t1 + 4 * w + g) * 2;
    u32 a_uk = L_UK + w * 4096 + n * 256 + ((g ^ n) << 4);
    PIN(a_qw); PIN(a_qr); PIN(a_pub); PIN(a_x); PIN(a_xb); PIN(a_x0); PIN(a_x1); PIN(a_uk);
    SB();
    VMW(WD);
    bar();
    ep_use();
    if (w == 1 && lane == 0) st8((v2u){tlo, thi}, rws, 0, WS_FL + wi * 8);
    u32 x0, x1;
    {
      const v4u xa = lds_ld<v4u>(a_x);
      const u32 xb = lds_ld<u32>(a_xb), xg0 = lds_ld<unsigned short>(a_x0), xg1 = lds_ld<unsigned short>(a_x1);
      SB();
      float mb, rs;
      lnstat(xa, xb, mb, rs);
      const u32 dp = pk(bflo(xg0) - mb, bflo(xg1) - mb);
      const float a0 = bflo(dp) * rs, a1 = bfhi(dp) * rs;
      x0 = pk(a0, a0); x1 = pk(a1, a1);
    }
    PIN(x0); PIN(x1);
    SB();
    STAMP(4);
    v4f qa = {0.f, 0.f, 0.f, 0.f};
    sfor<0, 24>([&](auto ic) {
      constexpr int i = ic, t = i < UP ? i : i - 4;
      constexpr bool isq = i < UP || i >= UP + 4;
      v4u oh;
      if constexpr (isq) { oh = onehot<(t & 15)>(t < 16 ? x0 : x1, msk); PIN(oh); }
      VMW(i + WD <= 24 ? WD - 1 : 23 - i);
      if constexpr (i + WD < 24) fld(std::integral_constant<int, i + WD>{});
      if constexpr (isq) { PIN(wq[t]); qa = mfma(oh, wq[t], qa); }
      if constexpr (i == UP + 3) {
#pragma unroll
        for (int u = 0; u < 4; ++u) wuk[u] = lds_ld<v4u>(a_uk ^ (u << 6));
      }
      if constexpr (i == 0) STAMP(3);
      if constexpr (i + WD == 23) STAMP(5);
      SB();
    });
    if (g < 2) lds_st<v2u>(a_qw, (v2u){pk(qa.x, qa.y), pk(qa.z, qa.w)});
    STAMP(6);
    bar();
    {
      v4u qf[4];
#pragma unroll
      for (int u = 0; u < 4; ++u) qf[u] = lds_ld<v4u>(a_qr + u * 64);
      v4f qb = {0.f, 0.f, 0.f, 0.f};
#pragma unroll
      for (int u = 0; u < 4; ++u) qb = mfma(qf[u], wuk[u], qb);
      float v = (qb.x + qb.y) + (qb.z + qb.w);
      v = sw16add(v, v) * (0.08838834764831845f * 1.4426950408889634f);
      const u32 p = pk(v, dpp<0xb1>(v));
      if ((lane & 0x31) == 0) __builtin_amdgcn_raw_buffer_store_b32(min(p, ~1u), rws, a_pub, 0, 16);
    }
    SB();
    STAMP(7);
    const rsrc_t rwuv = mk(pwuv, 512u * 4096 * 2), rwo = mk(pwo, 4096u * 640 * 2);
    const u32 uv_v = g * 8192 + ((n ^ ((((w & 3) << 1) | (g & 1)) << 1)) << 4), wo_v = g * 1280 + ((n ^ (((w & 1) * 4 + g) << 1)) << 4);
    const u32 q4 = n >> 2, p4 = n & 3;
    u32 a_wo[5], a_uv = L_UV + (16 * q4 + g) * 256 + ((w ^ ((q4 << 1) | (g & 1))) << 5) + p4 * 8;
#pragma unroll
    for (int k = 0; k < 5; ++k) {
      const u32 rho = 5 * w + (k < 4 ? p4 : 4), i = k < 4 ? k : p4;
      const u32 c4 = (rho >> 1) * 8 + 2 * i + (rho & 1);
      a_wo[k] = L_WO + (g * 5 + (c4 >> 5)) * 1024 + q4 * 256 + ((((c4 >> 2) & 7) ^ ((g & 1) * 4 + q4)) << 5) + (c4 & 3) * 8;
      PIN(a_wo[k]);
    }
    u32 a_cw = L_CP + w * 320 + n * 16 + g * 8, a_cr = L_CP + (n & 7) * 320 + g * 16, a_mr = L_ML + (g & 1) * 16;
    u32 a_xw = L_CX + ((((w >> 1) - cq) & 3) * 32 + (n >> 2) * 8 + (w & 1) * 4 + (n & 3)) * 2, a_xr = L_CX + g * 16;
    u32 a_in = g * OSK + n * 16, a_so = (lane >> 2) * 16384 + (lane & 3) * 16, a_ml = (4 * (n & 3) + g) * 512;
    u32 a_cs = (4 * (n & 3) + g) * 2048 + (n >> 3) * 256 + (((h >> 2) & 3) * 16 + (h & 1) * 8 + (n & 4) + (((h >> 4) ^ (n >> 3)) & 1) * 2 + ((h >> 1) & 1)) * 4;
    PIN(a_uv); PIN(a_cw); PIN(a_cr); PIN(a_mr); PIN(a_xw); PIN(a_xr); PIN(a_in); PIN(a_so); PIN(a_ml); PIN(a_cs);
    v4u uv[4], wo[4][5];
    SB();
    if (BK_SLEEP) __builtin_amdgcn_s_sleep(BK_SLEEP);
    static_assert(9 + BD <= 19 && UVP % 5 == 0 && UVP >= 5 && UVP <= 15, "quarter 3 reuses the slot of quarter 0");
    sfor<0, 24 + BD>([&](auto ic) {
      constexpr int i = ic;
      if constexpr (i >= BD) VMW(i < 24 ? BD - 1 : 23 - (i - BD));
      if constexpr (i >= 24) {
      } else if constexpr (i >= UVP && i < UVP + 4) {
        dma16(rwuv, uv_v, (128 * (w & 3) + 32 * cq + 16 * (w >> 2) + 4 * (i - UVP)) * 8192 + h * 256, L_UV + (4 * w + i - UVP) * 1024);
      } else {
        constexpr int j = i < UVP ? i : i - 4, u = j / 5, mb = j % 5;
        dma16(rwo, wo_v, (h * 128 + ((u + cq) & 3) * 32 + 4 * w) * 1280 + mb * 256, L_WO + (((u % 3) * 8 + w) * 5 + mb) * 1024);
      }
      constexpr int d = i - BD, dj = d < UVP ? d : d - 4;
      if constexpr (i == 0) STAMPA(14);
      if constexpr (i == BD - 1) STAMPA(2);
      if constexpr (d == UVP + 3) {
        STAMPA(13);
        bar();
#pragma unroll
        for (int u = 0; u < 4; ++u) uv[u] = trd2(a_uv + (64 * (u >> 1) + 4 * (u & 1)) * 256, a_uv + (64 * (u >> 1) + 4 * (u & 1) + 8) * 256);
        SB();
      } else if constexpr (d >= 0 && !(d >= UVP && d < UVP + 4) && dj % 5 == 4) {
        constexpr int u = dj / 5;
        if constexpr (u < 3) STAMPA(10 + u);
        bar();
        if constexpr (u == 3) STAMP(8);
#pragma unroll
        for (int k = 0; k < 5; ++k) wo[u][k] = trd2(a_wo[k] + (u % 3) * 40960, a_wo[k] + (u % 3) * 40960 + 20480);
        SB();
      }
    });
#pragma unroll
    for (int u = 0; u < 4; ++u) {
      PIN(uv[u]);
#pragma unroll
      for (int k = 0; k < 5; ++k) PIN(wo[u][k]);
    }
    SB();
    STAMP(9);
    const u32 s_ml = WS_ML + w * 8192 + h * 16, s_cs = WS_CS + w * 32768 + cq * 512, s_in = WS_IN3 + h * OSH + cq * OSC + 16 * w * OSK;
    const u32 s_i4[4] = {s_in, s_in + 4 * OSK, s_in + 8 * OSK, s_in + 12 * OSK};
    v4u ml, pv[4], af[4];
    asm volatile("" : "=v"(ml));
    {
      u64 bad = 0x000f000f000f000full;
      u32 sp = 0;
      _Pragma("nounroll") do {
        if (ML_SLEEP) __builtin_amdgcn_s_sleep(ML_SLEEP);
        ldpoll1(ml, bad, rws, a_ml, s_ml);
        bad &= bal((ml.z != tlo) | (ml.w != thi));
      } while (bad && ++sp < SPIN_CAP);
    }
    STAMPB(2);
    if (P_SLEEP) __builtin_amdgcn_s_sleep(P_SLEEP);
#pragma unroll
    for (int i = 0; i < 4; ++i) pv[i] = ld16a(rws, a_in, s_i4[i]);
    u32 cv = ld4a(rws, a_cs, s_cs);
    SB();
    {
      float mw = mx(uf(ml.x), dpp<0xb1>(uf(ml.x)));
      mw = mx(mw, dpp<0x4e>(mw));
      { auto q = __builtin_amdgcn_permlane16_swap(fu(mw), fu(mw), false, false); mw = mx(uf(q[0]), uf(q[1])); }
      { auto q = __builtin_amdgcn_permlane32_swap(fu(mw), fu(mw), false, false); mw = mx(uf(q[0]), uf(q[1])); }
      const float e = __builtin_amdgcn_exp2f(uf(ml.x) - mw);
      const u32 ee = pk(e, e);
      float lw = bfhi(ee) * uf(ml.y);
      lw += dpp<0xb1>(lw); lw += dpp<0x4e>(lw);
      lw = sw16add(lw, lw); lw = sw32add(lw, lw);
      if (lane < 2) lds_st<float>(L_ML + w * 4 + lane * 32, lane ? lw : mw);
      af[0] = onehot<0>(ee, msk); af[1] = onehot<1>(ee, msk); af[2] = onehot<2>(ee, msk); af[3] = onehot<3>(ee, msk);
#pragma unroll
      for (int i = 0; i < 4; ++i) PIN(af[i]);
    }
    SB();
    v4f c;
    u64 bad;
    auto ochk = [&](auto first) {
      u32 t[4];
      c = (v4f){0.f, 0.f, 0.f, 0.f};
      sfor<0, 4>([&](auto ic) {
        constexpr int i = ic;
        if constexpr (first) VMW(4 - i);
        PIN(pv[i]);
        c = mfma(af[i], pv[i], c);
        t[i] = sum4(pv[i]);
        t[i] += dppu<0xb1>(t[i]);
        if constexpr (i & 1) { t[i] = (n & 1) ? t[i] : t[i - 1]; t[i] += dppu<0x4e>(t[i]); }
        if constexpr (first) SB();
      });
      if constexpr (first) VMW(0);
      PIN(cv);
      bad = bal(((n & 2) ? t[3] : t[1]) + tgh != cv);
    };
    ochk(std::true_type{});
    if (__builtin_expect(bad != 0, 0)) {
      u32 sp = 0;
      _Pragma("nounroll") do {
        u64 bo[4];
#pragma unroll
        for (int i = 0; i < 4; ++i) bo[i] = ((bad >> i) & 0x1111111111111111ull) * 15;
        ldpoll(pv, cv, bo, bad, rws, a_in, s_i4, a_cs, s_cs);
        ochk(std::false_type{});
        CNT(nsp3);
      } while (bad && ++sp < SPIN_CAP);
    }
    STAMPB(10);
    if (g < 2) lds_st<v2u>(a_cw, (v2u){pk(c.x, c.y), pk(c.z, c.w)});
    STAMPB(11);
    bar();
    {
      v4u cf[4];
#pragma unroll
      for (int u = 0; u < 4; ++u) cf[u] = lds_ld<v4u>(a_cr + u * 64);
      const v4f m4 = lds_ld<v4f>(a_mr), l4 = lds_ld<v4f>(a_mr + 32);
      v4f cx = {0.f, 0.f, 0.f, 0.f};
#pragma unroll
      for (int u = 0; u < 4; ++u) cx = mfma(cf[u], uv[u], cx);
      const float mg = sw16max(fmaxf(fmaxf(m4.x, m4.y), fmaxf(m4.z, m4.w)));
      const float e0 = __builtin_amdgcn_exp2f(m4.x - mg), e1 = __builtin_amdgcn_exp2f(m4.y - mg);
      const float e2 = __builtin_amdgcn_exp2f(m4.z - mg), e3 = __builtin_amdgcn_exp2f(m4.w - mg);
      float den = (e0 * l4.x + e1 * l4.y) + (e2 * l4.z + e3 * l4.w);
      const float inv = __builtin_amdgcn_rcpf(sw16add(den, den));
      float v = (e0 * cx.x + e1 * cx.y) + (e2 * cx.z + e3 * cx.w);
      v = sw16add(v, v) * inv;
      if (g == 0) lds_st<unsigned short>(a_xw, (unsigned short)pk(v, v));
    }
    STAMPB(12);
    bar();
    {
      v4u xf[4];
#pragma unroll
      for (int u = 0; u < 4; ++u) xf[u] = lds_ld<v4u>(a_xr + u * 64);
      v4f s[5];
#pragma unroll
      for (int k = 0; k < 5; ++k) s[k] = (v4f){0.f, 0.f, 0.f, 0.f};
#pragma unroll
      for (int u = 0; u < 4; ++u)
#pragma unroll
        for (int k = 0; k < 5; ++k) s[k] = mfma(xf[u], wo[u][k], s[k]);
      const u32 s4 = fu(s[4].x);
      const v4f d = {uf(__builtin_amdgcn_update_dpp(fu(s[0].x), s4, 0xe4, 0x2, 0xf, false)), uf(__builtin_amdgcn_update_dpp(fu(s[1].x), s4, 0x104, 0x2, 0xf, false)),
                     uf(__builtin_amdgcn_update_dpp(fu(s[2].x), s4, 0x108, 0x2, 0xf, false)), uf(__builtin_amdgcn_update_dpp(fu(s[3].x), s4, 0x10c, 0x2, 0xf, false))};
      if (lane < 20) st16(tagged(d, tlo), rws, a_so, WS_IN5 + (5 * w * 256 + wi) * 64);
    }
    STAMPB(13);
  } else {
    const u32 ks = KSW & 1 ? xcd * 16 + r - 16 : b - 128, kc = KSW & 2 ? xcd * 16 + r - 16 : b - 128;
    const rsrc_t rck = mk(pckv, 16384u * 1024);
    u32 a_o = (4 * g + (n >> 3)) * OSH + (n & 7) * 16;
    const u32 s_o = WS_IN3 + (w >> 1) * OSC + ks * OSK + (w & 1) * 128;
    if (w == 0) { dma16(rx, lane * 16, 0, L_XK); dma16(rx, lane * 16, 1024, L_XK + 1024); }
    const u32 fb = xcd + 8 * ((r >> 2) & 3), yi = fb * 4 + (r & 3);
    float xs[4];
    const rsrc_t rwuk = mk(pwuk, 512u * 4096 * 2);
    const u32 ck_v = g * 1024 + ((n ^ (g << 2)) << 4), ck_s = (kc * 128 + 16 * w) * 1024;
    u32 ra[4], rq[4], va[2], a_pr[4], a_mx, a_pw, a_f1, a_f2;
    v4u kf[16], vt[4][4];
    auto stage = [&](auto sc) {
      constexpr int s = sc;
      bar();
#pragma unroll
      for (int u = 0; u < 4; ++u) kf[4 * s + u] = lds_ld<v4u>(L_CKV + ra[u] + w * 4096 + s * 32768);
#pragma unroll
      for (int kk = 0; kk < 4; ++kk) vt[kk][s] = trd2(va[0] + s * 32768 + kk * 8192, va[1] + s * 32768 + kk * 8192);
      if constexpr (s == 0) {
        ep_use();
        const v4u xa = lds_ld<v4u>(L_XK + lane * 16);
        const u32 xb = lds_ld<u32>(L_XK + 1024 + lane * 4);
        float mb, rs;
        lnstat(xa, xb, mb, rs);
#pragma unroll
        for (int i = 0; i < 4; ++i) {
          xs[i] = rb(rb(bflo(lds_ld<unsigned short>(L_XK + w * 256 + i * 64 + (lane & 62))) - mb) * rs);
          PIN(xs[i]);
        }
      }
      SB();
    };
    auto ckld = [&](int s, int j) { dma16s(rck, ck_v ^ (j << 4), ck_s + j * 4096 + s * 256, L_CKV + s * 32768 + (16 * w + 4 * j) * 256); };
    static_assert(KD >= 5 && KD <= 8 && QW < KD, "");
    constexpr int NA = PF + 17, IF = PF + FPOS;
    static_assert(IF < NA - QW && FPOS > 8, "");
    v2u fv;
    sfor<0, NA>([&](auto ic) {
      constexpr int i = ic;
      if constexpr (i >= KD) VMW(KD - 1);
      if constexpr (i == PF && KS) __builtin_amdgcn_s_sleep(KS);
      if constexpr (i < PF) dma16(rwuk, lane * 16, ((r - 16) * 32 + 4 * w + i) * 8192 + xcd * 1024, L_PFD + w * 1024);
      else if constexpr (i == IF) fv = ld8a(rws, n * 8, WS_FL + w * 128);
      else { constexpr int k = i - PF - (i > IF); ckld(k >> 2, k & 3); }
      if constexpr (i == KD - 1) {
        SB();
        STAMP(1);
        ep_load();
        const u32 q = n >> 2, p = n & 3;
#pragma unroll
        for (int u = 0; u < 4; ++u) { ra[u] = n * 256 + (((g * 4 + u) ^ sig(n)) << 4); PIN(ra[u]); }
#pragma unroll
        for (int t = 0; t < 2; ++t) {
          const u32 row = 8 * g + 4 * t + q;
          va[t] = L_CKV + row * 256 + (((2 * w + (p >> 1)) ^ sig(row & 15)) << 4) + 8 * (p & 1);
          PIN(va[t]);
        }
        a_mx = L_MX + n * 4; a_pw = L_P + n * 256 + (((2 * w + (g >> 1)) ^ n) << 4) + (g & 1) * 8;
#pragma unroll
        for (int kk = 0; kk < 4; ++kk) { a_pr[kk] = L_P + (w & 1) * 4096 + n * 256 + (((4 * kk + g) ^ n) << 4); PIN(a_pr[kk]); }
#pragma unroll
        for (int u = 0; u < 4; ++u) { rq[u] = ra[u] + L_QA; PIN(rq[u]); }
        a_f1 = (lane >> 1) * 3456 + (lane & 1) * 16; a_f2 = w < 4 ? (lane >> 3) * 3456 + (lane & 7) * 16 : lane * 16;
        PIN(a_mx); PIN(a_pw); PIN(a_o); PIN(a_f1); PIN(a_f2);
        SB();
      }
      if constexpr (i - KD == PF + 3) stage(std::integral_constant<int, 0>{});
      if constexpr (i - KD == PF + 7) stage(std::integral_constant<int, 1>{});
    });
    const rsrc_t rf = mk(w < 4 ? pw1 : pw2, fb < 27 && (FFP == 0 || w >= 4) ? 640u * 1728 * 2 : 0u);
    const u32 ff_s = w < 4 ? (160 * (r & 3) + 40 * w) * 3456 + fb * 128 : yi * 20480 + (w - 4) * 5120, ff_d = w < 4 ? 8 * 3456 : 1024;
    auto ffn_ld = [&](int i) { dma16(rf, a_f2, ff_s + i * ff_d, L_FF + w * 5120 + i * 1024); };
    if (w == 0 && lane < 32) lds_st<float>(L_MX + lane * 4, -__builtin_inff());
    u32 a_q = lane * 16, a_qs = L_QA + w * 1024 + lane * 16;
    u32 a_lw = L_L + ((g >> 1) * 16 + n) * 48 + w * 4, a_tl = L_L + ((w & 1) * 16 + n) * 48, a_ts = WS_CS + ks * 2048 + w * 256 + lane * 4;
    u32 a_ms = WS_ML + ks * 512 + (w & 1) * 256 + n * 16;
    PIN(a_q); PIN(a_qs); PIN(a_tl); PIN(a_ts); PIN(a_lw); PIN(a_ms);
    SB();
    if constexpr (QW < 4) { VMW(FPOS > 12 ? 5 : 4); stage(std::integral_constant<int, 2>{}); }
    VMW(QW);
    if (QS) __builtin_amdgcn_s_sleep(QS);
    v4u qr[4];
#pragma unroll
    for (int j = 0; j < 4; ++j) qr[j] = ld16a(rws, a_q, WS_QA + j * 8192 + w * 1024);
    STAMP(2);
    PIN(fv);
    u64 bf = bal((fv.x != tlo) | (fv.y != thi));
    if constexpr (QW >= 4) { VMW(8); stage(std::integral_constant<int, 2>{}); }
    if constexpr (S3B) { VMW(4); stage(std::integral_constant<int, 3>{}); }
    STAMP(3);
    u64 bq[4];
    auto qchk = [&](int j) {
      PIN(qr[j]);
      lds_st<v4u>(a_qs + j * 8192, qr[j]);
      bq[j] = bal(max(max(qr[j].x, qr[j].y), max(qr[j].z, qr[j].w)) == ~0u);
    };
    sfor<0, 4>([&](auto jc) { VMW(3 - jc); qchk(jc); SB(); });
    STAMP(4);
    if (__builtin_expect((bf | bq[0] | bq[1] | bq[2] | bq[3]) != 0, 0)) {
      const u32 s_q[4] = {WS_QA + w * 1024, WS_QA + 8192 + w * 1024, WS_QA + 16384 + w * 1024, WS_QA + 24576 + w * 1024};
      u32 sp = 0;
      if (bf) {
        _Pragma("nounroll") do {
          asm volatile("" ::: "memory");
          fv = __builtin_amdgcn_raw_buffer_load_b64(rws, n * 8, WS_FL + w * 128, 16);
        } while (bal((fv.x != tlo) | (fv.y != thi)) && ++sp < SPIN_CAP);
        bq[0] = bq[1] = bq[2] = bq[3] = ~0ull;
      }
      _Pragma("nounroll") do {
        if (QRS) __builtin_amdgcn_s_sleep(QRS);
        ldpoll4(qr, bq, rws, a_q, s_q);
#pragma unroll
        for (int j = 0; j < 4; ++j) qchk(j);
        CNT(nsp2);
      } while ((bq[0] | bq[1] | bq[2] | bq[3]) && ++sp < SPIN_CAP);
    }
    bar();
    STAMP(5);
    v4f sc[2] = {{0.f, 0.f, 0.f, 0.f}, {0.f, 0.f, 0.f, 0.f}};
    {
      v4u qf[32];
      auto qrd = [&](auto jc) { constexpr int j = jc, u = j >> 1; qf[j] = lds_ld<v4u>(rq[u & 3] + (j & 1) * 4096 + (u >> 2) * 8192); };
      sfor<0, SD>(qrd);
      SB();
      sfor<0, 32>([&](auto jc) {
        constexpr int j = jc;
        sc[j & 1] = mfma(kf[j >> 1], qf[j], sc[j & 1]);
        if constexpr (j + SD < 32) qrd(std::integral_constant<int, j + SD>{});
        if constexpr (FFP < 2 && j % FSP == 0 && j / FSP >= 1 && j / FSP <= 5) ffn_ld(j / FSP - 1);
        if constexpr (!S3B && j + SD == 32) {
#pragma unroll
          for (int u = 0; u < 4; ++u) kf[12 + u] = lds_ld<v4u>(L_CKV + ra[u] + w * 4096 + 3 * 32768);
        }
        SB();
      });
    }
    STAMP(6);
#pragma unroll
    for (int ht = 0; ht < 2; ++ht)
      __hip_atomic_fetch_max((LAS float*)(a_mx + ht * 64), mx(mx(sc[ht].x, sc[ht].y), mx(sc[ht].z, sc[ht].w)), __ATOMIC_RELAXED,
                             __HIP_MEMORY_SCOPE_WORKGROUP);
    STAMP(7);
    bar();
    float mrow[2], ls[2];
#pragma unroll
    for (int ht = 0; ht < 2; ++ht) mrow[ht] = lds_ld<float>(a_mx + ht * 64);
    if constexpr (!S3B) {
#pragma unroll
      for (int kk = 0; kk < 4; ++kk) vt[kk][3] = trd2(va[0] + 3 * 32768 + kk * 8192, va[1] + 3 * 32768 + kk * 8192);
    }
    SB();
#pragma unroll
    for (int ht = 0; ht < 2; ++ht) {
      const float p0 = __builtin_amdgcn_exp2f(sc[ht].x - mrow[ht]), p1 = __builtin_amdgcn_exp2f(sc[ht].y - mrow[ht]);
      const float p2 = __builtin_amdgcn_exp2f(sc[ht].z - mrow[ht]), p3 = __builtin_amdgcn_exp2f(sc[ht].w - mrow[ht]);
      lds_st<v2u>(a_pw + ht * 4096, (v2u){pk(p0, p1), pk(p2, p3)});
      ls[ht] = (p0 + p1) + (p2 + p3);
    }
    {
      const float x = sw32add(ls[0], ls[1]);
      lds_st<float>(a_lw, sw16add(x, x));
    }
    STAMP(8);
    if (w >= 4) __builtin_amdgcn_s_setprio(1);
    bar();
    v4u pf[2][4];
#pragma unroll
    for (int ht = 0; ht < 2; ++ht)
#pragma unroll
      for (int kk = 0; kk < 4; ++kk) pf[ht][kk] = lds_ld<v4u>(a_pr[kk] ^ (ht * 4096));
    u32 cs[2];
    auto mlst = [&] {
      if ((w >> 1) == MLW) {
        const v4f l4 = lds_ld<v4f>(a_tl) + lds_ld<v4f>(a_tl + 16);
        if (g == 0) st16((v4u){fu((w & 1) ? mrow[1] : mrow[0]), fu((l4.x + l4.y) + (l4.z + l4.w)), tlo, thi}, rws, a_ms, 0);
      }
    };
    if (MLP == 0) mlst();
    SB();
    STAMPA(10);
#pragma unroll
    for (int ht = 0; ht < 2; ++ht) {
      v4f o[4];
#pragma unroll
      for (int ct = 0; ct < 4; ++ct) o[ct] = (v4f){0.f, 0.f, 0.f, 0.f};
#pragma unroll
      for (int kk = 0; kk < 4; ++kk)
#pragma unroll
        for (int ct = 0; ct < 4; ++ct) {
          o[ct] = mfma(pf[ht][kk], vt[kk][ct], o[ct]);
        }
      if (MLP == 1 + 2 * ht) mlst();
      STAMPA(11 + 2 * ht);
#pragma unroll
      for (int pr = 0; pr < 2; ++pr) {
        const u32 x0 = pk(o[0][2 * pr], o[1][2 * pr]), x1 = pk(o[2][2 * pr], o[3][2 * pr]);
        const u32 y0 = pk(o[0][2 * pr + 1], o[1][2 * pr + 1]), y1 = pk(o[2][2 * pr + 1], o[3][2 * pr + 1]);
        const v4u d = {(u32)__builtin_amdgcn_update_dpp(x0, y0, 0x128, 0xf, 0xc, false), (u32)__builtin_amdgcn_update_dpp(x1, y1, 0x128, 0xf, 0xc, false),
                       (u32)__builtin_amdgcn_update_dpp(y0, x0, 0x128, 0xf, 0x3, false), (u32)__builtin_amdgcn_update_dpp(y1, x1, 0x128, 0xf, 0x3, false)};
        st16(d, rws, a_o, s_o + (16 * (ht ^ (w & 1)) + 2 * pr) * OSH);
        u32 t = sum4(d);
        t += dppu<0xb1>(t);
        cs[ht] = pr && !(n & 1) ? cs[ht] : t;
      }
      cs[ht] += dppu<0x4e>(cs[ht]);
      if (MLP == 2 + 2 * ht) mlst();
      STAMPA(12 + 2 * ht);
      if (PVS) SB();
    }
    __builtin_amdgcn_raw_buffer_store_b32(((n & 2) ? cs[1] : cs[0]) + tgh, rws, a_ts, 0, 16);
    STAMP(9);
    if (fb < 27) {
      if (FBAR) __builtin_amdgcn_s_barrier();
      if (FFP == 2 && w >= 4) {
#pragma unroll
        for (int i = 0; i < 5; ++i) ffn_ld(i);
      }
      if (w >= 5) VMW(FFP == 2 ? 0 : 5);
      else {
        v4u w1r[4], w2r[4];
        const rsrc_t rw1 = mk(pw1, 640u * 1728 * 2);
#pragma unroll
        for (int i = 0; i < 4; ++i) w1r[i] = ld16p(rw1, a_f1, (128 * w + 32 * i) * 3456 + yi * 32);
        VMW(0);
#pragma unroll
        for (int i = 0; i < 4; ++i) PIN(w1r[i]);
        STAMPB(10);
        float ha[8];
#pragma unroll
        for (int e = 0; e < 8; ++e) ha[e] = 0.f;
#pragma unroll
        for (int i = 0; i < 4; ++i) {
#pragma unroll
          for (int q = 0; q < 4; ++q) { ha[2 * q] += xs[i] * bflo(w1r[i][q]); ha[2 * q + 1] += xs[i] * bfhi(w1r[i][q]); }
        }
        float h0 = sw16add(sw32add(ha[0], ha[2]), sw32add(ha[1], ha[3]));
        float h1 = sw16add(sw32add(ha[4], ha[6]), sw32add(ha[5], ha[7]));
        h0 += dpp<0x128>(h0); h1 += dpp<0x128>(h1); h0 += dpp<0x124>(h0); h1 += dpp<0x124>(h1); h0 += dpp<0x122>(h0); h1 += dpp<0x122>(h1);
        if (n < 4) lds_st<float>(L_QA + w * 64 + ((n & 1) * 8 + (n >> 1) * 4 + g) * 4, n < 2 ? h0 : h1);
        STAMPB(11);
        bar();
#pragma unroll
        for (int i = 0; i < 4; ++i) w2r[i] = lds_ld<v4u>(L_FF + 20480 + (4 * i + g) * 1280 + w * 256 + n * 16);
        float hs = 0.f;
#pragma unroll
        for (int v = 0; v < 5; ++v) hs += lds_ld<float>(L_QA + v * 64 + (4 * (n & 3) + g) * 4);
        hs = fmaxf(hs, 0.f);
        const u32 hh = pk(hs, hs);
        u32 msk[4];
#pragma unroll
        for (int q = 0; q < 4; ++q) msk[q] = (q == (int)((n & 7) >> 1)) ? ((n & 1) ? 0xffff0000u : 0x0000ffffu) : 0u;
        v4f y = mfma(onehot<0>(hh, msk), w2r[0], (v4f){0.f, 0.f, 0.f, 0.f});
        y = mfma(onehot<1>(hh, msk), w2r[1], y);
        y = mfma(onehot<2>(hh, msk), w2r[2], y);
        y = mfma(onehot<3>(hh, msk), w2r[3], y);
        if (g < 2) st16(tagged(y, tlo), rws, ((n >> 2) * 2 + g) * 16384 + (n & 3) * 16, WS_IN5 + (w * 8 * 256 + 128 + yi) * 64);
        STAMPB(12);
      }
    } else if (w < 2) {
      const u32 rho = (yi - 108) * 2 + w;
      const u32 kg = (g & 1) * 2 + (g >> 1);
      const u32 col = (rho >> 1) * 32 + 8 * kg + 4 * (rho & 1) + (n & 3);
      const u32 xo = __builtin_amdgcn_raw_buffer_load_b16(rx, col * 2, 0, 0);
      float gt;
      {
        v4u yv[7];
        __builtin_amdgcn_s_sleep(Y_SLEEP);
        _Pragma("nounroll") for (u32 sp = 0;; ++sp) {
          asm volatile("" ::: "memory");
#pragma unroll
          for (int i = 0; i < 7; ++i) yv[i] = ld16<16>(rws, (i == 6 && lane >= 48 ? lane - 48 : lane) * 16 + (i & 3) * 1024, WS_IN5 + rho * 16384 + 8192 + (i >> 2) * 4096);
          bool ok = true;
#pragma unroll
          for (int i = 0; i < 7; ++i) ok &= tagof(yv[i]) == tlo;
          if (__all(ok) || sp >= SPIN_CAP) break;
          __builtin_amdgcn_s_sleep(4);
        }
        v4f a = lane < 48 ? untag(yv[6]) : (v4f){0.f, 0.f, 0.f, 0.f};
#pragma unroll
        for (int i = 0; i < 6; ++i) a += untag(yv[i]);
        float y = sw16add(sw32add(a.x, a.y), sw32add(a.z, a.w));
        y += dpp<0x128>(y); y += dpp<0x124>(y);
        const u32 n3 = n & 3;
        y = uf(__builtin_amdgcn_ds_bpermute((int)(((((n3 & 1) * 2 + (n3 >> 1)) << 4) + kg) * 4), fu(y)));
        y = rb(y);
        gt = rb(y / (1.0f + __expf(-y)));
      }
      float xf = bflo(xo);
      PIN(gt); PIN(xf);
      STAMPB(13);
      __builtin_amdgcn_s_sleep(RED_SLEEP);
      v4u sv[8];
      _Pragma("nounroll") for (u32 sp = 0;; ++sp) {
        asm volatile("" ::: "memory");
#pragma unroll
        for (int i = 0; i < 8; ++i) sv[i] = ld16<16>(rws, lane * 16 + (i & 3) * 1024, WS_IN5 + rho * 16384 + (i >> 2) * 4096);
        bool ok = true;
#pragma unroll
        for (int i = 0; i < 8; ++i) ok &= tagof(sv[i]) == tlo;
        if (__all(ok) || sp >= SPIN_CAP) break;
        CNT(nsp5);
      }
      STAMPB(14);
      v4f a = untag(sv[0]);
#pragma unroll
      for (int i = 1; i < 8; ++i) a += untag(sv[i]);
      float s = sw16add(sw32add(a.x, a.y), sw32add(a.z, a.w));
      s += dpp<0x128>(s); s += dpp<0x124>(s);
      const u32 o = pk(xf + rb(rb(s) * gt), 0.f);
      if (n < 4) ((unsigned short*)pout)[col] = (unsigned short)o;
      if (rho == 0 && lane == 0) __builtin_amdgcn_raw_buffer_store_b64((v2u){tlo, thi}, rws, 0, 0, 16);
    } else {
      VMW(5);
    }
  }
  STAMP(15);
  if (lane == 0) {
    u32* o = (u32*)((char*)pws + WS_DBG) + (b * 8 + w) * 32;
#pragma unroll
    for (int i = 0; i < 16; ++i) o[i] = stamp[i];
    o[19] = (u32)__builtin_amdgcn_s_memtime() - clk0;
    o[16] = nsp2; o[17] = nsp3; o[18] = nsp5;
  }
}

__global__ void kspin(const unsigned* ts) {
  unsigned t0 = ts[0]; int mn = 0, mx = -100000000; long long sm = 0; int cn = 0;
  for (int i = threadIdx.x; i < 2048; i += 64) { int a = (int)(ts[i * 32] - t0); mn = a < mn ? a : mn; unsigned e = ts[i * 32 + WHICH];
    bool in = CLSX == 2 || (i >= 1024) == (CLSX == 1);
    if (e && in) { int d = (int)(e - t0); mx = d > mx ? d : mx; sm += d; ++cn; } }
  for (int o = 32; o; o >>= 1) { int a = __shfl_xor(mn, o), e = __shfl_xor(mx, o); mn = a < mn ? a : mn; mx = e > mx ? e : mx; sm += __shfl_xor(sm, o); cn += __shfl_xor(cn, o); }
  long long d = MEAN ? (sm * KMUL) / (cn ? cn : 1) - (long long)mn * KMUL : (long long)(mx - mn) * KMUL; if (d > 100000) d = 100000; if (d < 0) d = 0;
  unsigned long long s = __builtin_amdgcn_s_memrealtime(); while ((long long)(__builtin_amdgcn_s_memrealtime() - s) < d) __builtin_amdgcn_s_sleep(2);
}

extern "C" void kernel_launch(void* const* d_in, const int* in_sizes, int n_in, void* d_out, int out_size, void* d_ws, size_t ws_size,
                              hipStream_t stream) {
  mla_decode_kernel<<<256, 512, 0, stream>>>(d_in[0], d_in[1], d_in[2], d_in[3], d_ws, d_in[4], d_in[5], d_in[7], d_in[8], d_out);
  kspin<<<1, 64, 0, stream>>>((const unsigned*)((char*)d_ws + WS_DBG));
}
```

```cpp
#define STAMPS
#define STAMPMASK 0x8001
#define MODE 0
#include <hip/hip_runtime.h>
#include <type_traits>

typedef unsigned u32;
typedef unsigned long long u64;
typedef u32 v4u __attribute__((ext_vector_type(4)));
typedef u32 v2u __attribute__((ext_vector_type(2)));
typedef float v4f __attribute__((ext_vector_type(4)));
typedef float v2f __attribute__((ext_vector_type(2)));
typedef __bf16 bfx8 __attribute__((ext_vector_type(8)));
typedef __bf16 bfx2 __attribute__((ext_vector_type(2)));
typedef short v4s __attribute__((ext_vector_type(4)));
typedef __amdgpu_buffer_rsrc_t rsrc_t;
#define LAS __attribute__((address_space(3)))
#define DEVI __device__ __forceinline__

#define WD 6
#define UP 10
#define FFP 0
#define UVP 5
#define BD 8
#define CODE_BYTES 16384
#define KD 8
#define S3B 1
#define QW 2
#define FSP 4
#define KSW 1
#define FPOS 12
#define QRS 0
#define OSH 1024
#define OSC 256
#define OSK 32768
#define MLP 1
#define MLW 0
#define QS 0
#define PVS 1
#define FBAR 1
#define KS 8
#define SD 12
#define PF 4
#define BK_SLEEP 16
#define RED_SLEEP 0
#define Y_SLEEP 8
#define ML_SLEEP 4
#define P_SLEEP 0
#define AUX_CKV " nt"

constexpr u32 WS_FL = 2048, WS_QA = 4096, WS_ML = 131072, WS_CS = 262144, WS_IN3 = 1u << 20, WS_IN5 = 6u << 20, WS_BYTES = 8u << 20;
constexpr u32 WS_DBG = 16u << 20;
constexpr u32 L_END = 163840;
constexpr u32 L_CKV = 0, L_QA = 131072, L_MX = 0, L_L = 256, L_P = 8192, L_FF = 16384, L_XK = L_QA, L_PFD = L_QA + 8192;
constexpr u32 L_X = 0, L_UK = 4096, L_WO = 0, L_UV = 122880, L_QP = 155648, L_CP = L_UV, L_ML = L_UV + 4096, L_CX = L_UV + 4352;
constexpr u32 SPIN_CAP = 100000;

DEVI rsrc_t mk(const void* p, u32 bytes) { return __builtin_amdgcn_make_buffer_rsrc(const_cast<void*>(p), 0, bytes, 0x00020000); }
template <int AUX> DEVI v4u ld16(rsrc_t r, u32 v, u32 s) { return __builtin_amdgcn_raw_buffer_load_b128(r, v, s, AUX); }
DEVI void st16(v4u d, rsrc_t r, u32 v, u32 s) {
  __builtin_amdgcn_raw_buffer_store_b128(d, r, v, s, 16);
  asm volatile("s_nop 1" ::"v"(d));
}
DEVI float bflo(u32 x) { return __builtin_bit_cast(float, x << 16); }
DEVI float bfhi(u32 x) { return __builtin_bit_cast(float, x & 0xffff0000u); }
DEVI u32 pk(float a, float b) { v2f t = {a, b}; return __builtin_bit_cast(u32, __builtin_convertvector(t, bfx2)); }
DEVI float rb(float a) { return bfhi(pk(a, a)); }
DEVI u32 fu(float a) { return __builtin_bit_cast(u32, a); }
DEVI float uf(u32 a) { return __builtin_bit_cast(float, a); }
template <int C> DEVI float dpp(float x) { return uf(__builtin_amdgcn_update_dpp(0, fu(x), C, 0xf, 0xf, true)); }
template <int C> DEVI u32 dppu(u32 x) { return __builtin_amdgcn_update_dpp(0, x, C, 0xf, 0xf, true); }
DEVI float rowsum(float x) { x += dpp<0x128>(x); x += dpp<0x124>(x); x += dpp<0x122>(x); x += dpp<0x121>(x); return x; }
DEVI float sw32add(float a, float b) { auto p = __builtin_amdgcn_permlane32_swap(fu(a), fu(b), false, false); return uf(p[0]) + uf(p[1]); }
DEVI float sw16add(float a, float b) { auto p = __builtin_amdgcn_permlane16_swap(fu(a), fu(b), false, false); return uf(p[0]) + uf(p[1]); }
DEVI float sw32max(float a) { auto p = __builtin_amdgcn_permlane32_swap(fu(a), fu(a), false, false); return fmaxf(uf(p[0]), uf(p[1])); }
DEVI float sw16max(float a) { auto p = __builtin_amdgcn_permlane16_swap(fu(a), fu(a), false, false); return fmaxf(uf(p[0]), uf(p[1])); }
DEVI v4f mfma(v4u a, v4u b, v4f c) { return __builtin_amdgcn_mfma_f32_16x16x32_bf16(__builtin_bit_cast(bfx8, a), __builtin_bit_cast(bfx8, b), c, 0, 0, 0); }
DEVI float dot2(u32 a, u32 b, float c) { return __builtin_amdgcn_fdot2_f32_bf16(__builtin_bit_cast(bfx2, a), __builtin_bit_cast(bfx2, b), c, false); }
DEVI u32 sig(u32 r) { return ((r & 3) << 2) | ((r >> 2) & 3); }
template <class T> DEVI T lds_ld(u32 a) { return *(LAS T*)a; }
template <class T> DEVI void lds_st(u32 a, T v) { *(LAS T*)a = v; }
DEVI v2u trd(u32 a) { return __builtin_bit_cast(v2u, __builtin_amdgcn_ds_read_tr16_b64_v4i16((LAS v4s*)a)); }
DEVI v4u trd2(u32 a, u32 b) { v2u x = trd(a), y = trd(b); return (v4u){x.x, x.y, y.x, y.y}; }
DEVI void bar() { asm volatile("s_waitcnt lgkmcnt(0)\n\ts_barrier" ::: "memory"); }
DEVI void dma16(rsrc_t r, u32 voff, u32 soff, u32 ldsdst) {
  asm volatile("s_mov_b32 m0, %3\n\ts_nop 0\n\tbuffer_load_dwordx4 %0, %1, %2 offen lds" ::"v"(voff), "s"(r), "s"(soff), "s"(ldsdst) : "memory");
}
DEVI void dma16s(rsrc_t r, u32 voff, u32 soff, u32 ldsdst) {
  asm volatile("s_mov_b32 m0, %3\n\ts_nop 0\n\tbuffer_load_dwordx4 %0, %1, %2 offen" AUX_CKV " lds" ::"v"(voff), "s"(r), "s"(soff), "s"(ldsdst) : "memory");
}
DEVI void st8(v2u d, rsrc_t r, u32 v, u32 s) { __builtin_amdgcn_raw_buffer_store_b64(d, r, v, s, 16); }
DEVI v4u ld16a(rsrc_t r, u32 voff, u32 soff) {
  v4u d;
  asm volatile("buffer_load_dwordx4 %0, %1, %2, %3 offen sc1" : "=v"(d) : "v"(voff), "s"(r), "s"(soff) : "memory");
  return d;
}
DEVI v4u ld16p(rsrc_t r, u32 voff, u32 soff) {
  v4u d;
  asm volatile("buffer_load_dwordx4 %0, %1, %2, %3 offen" : "=v"(d) : "v"(voff), "s"(r), "s"(soff) : "memory");
  return d;
}
DEVI v2u ld8a(rsrc_t r, u32 voff, u32 soff) {
  v2u d;
  asm volatile("buffer_load_dwordx2 %0, %1, %2, %3 offen sc1" : "=v"(d) : "v"(voff), "s"(r), "s"(soff) : "memory");
  return d;
}
DEVI u32 ld4a(rsrc_t r, u32 voff, u32 soff) {
  u32 d;
  asm volatile("buffer_load_dword %0, %1, %2, %3 offen sc1" : "=v"(d) : "v"(voff), "s"(r), "s"(soff) : "memory");
  return d;
}
DEVI void ldpoll(v4u* d, u32& t, const u64* m, u64 mt, rsrc_t r, u32 vd, const u32* sd, u32 vt, u32 st) {
  asm volatile("s_mov_b64 exec, %12\n\tbuffer_load_dwordx4 %0, %5, %7, %17 offen sc1\n\t"
               "s_mov_b64 exec, %13\n\tbuffer_load_dwordx4 %1, %5, %7, %9 offen sc1\n\t"
               "s_mov_b64 exec, %14\n\tbuffer_load_dwordx4 %2, %5, %7, %10 offen sc1\n\t"
               "s_mov_b64 exec, %15\n\tbuffer_load_dwordx4 %3, %5, %7, %11 offen sc1\n\t"
               "s_mov_b64 exec, %16\n\tbuffer_load_dword %4, %6, %7, %8 offen sc1\n\t"
               "s_mov_b64 exec, -1\n\ts_waitcnt vmcnt(0)"
               : "+v"(d[0]), "+v"(d[1]), "+v"(d[2]), "+v"(d[3]), "+v"(t)
               : "v"(vd), "v"(vt), "s"(r), "s"(st), "s"(sd[1]), "s"(sd[2]), "s"(sd[3]), "s"(m[0]), "s"(m[1]), "s"(m[2]), "s"(m[3]), "s"(mt), "s"(sd[0])
               : "memory");
}
DEVI void ldpoll4(v4u* d, const u64* m, rsrc_t r, u32 vd, const u32* sd) {
  asm volatile("s_mov_b64 exec, %10\n\tbuffer_load_dwordx4 %0, %4, %5, %6 offen sc1\n\t"
               "s_mov_b64 exec, %11\n\tbuffer_load_dwordx4 %1, %4, %5, %7 offen sc1\n\t"
               "s_mov_b64 exec, %12\n\tbuffer_load_dwordx4 %2, %4, %5, %8 offen sc1\n\t"
               "s_mov_b64 exec, %13\n\tbuffer_load_dwordx4 %3, %4, %5, %9 offen sc1\n\t"
               "s_mov_b64 exec, -1\n\ts_waitcnt vmcnt(0)"
               : "+v"(d[0]), "+v"(d[1]), "+v"(d[2]), "+v"(d[3])
               : "v"(vd), "s"(r), "s"(sd[0]), "s"(sd[1]), "s"(sd[2]), "s"(sd[3]), "s"(m[0]), "s"(m[1]), "s"(m[2]), "s"(m[3])
               : "memory");
}
DEVI void ldpoll1(v4u& d, u64 m, rsrc_t r, u32 vd, u32 sd) {
  asm volatile("s_mov_b64 exec, %4\n\tbuffer_load_dwordx4 %0, %1, %2, %3 offen sc1\n\ts_mov_b64 exec, -1\n\ts_waitcnt vmcnt(0)"
               : "+v"(d) : "v"(vd), "s"(r), "s"(sd), "s"(m) : "memory");
}
DEVI u64 bal(bool c) { return __builtin_amdgcn_ballot_w64(c); }
DEVI float mx(float a, float b) { return __builtin_elementwise_maximum(a, b); }
#define SB() __builtin_amdgcn_sched_barrier(0)
#define PIN(x) asm volatile("" : "+v"(x))
#define VMW(n) asm volatile("s_waitcnt vmcnt(%0)" ::"n"(n) : "memory")

#define STAMP(i) do { if (!((STAMPMASK >> (i)) & 1)) break; SB(); stamp[i] = (u32)__builtin_amdgcn_s_memrealtime(); asm volatile("s_waitcnt lgkmcnt(0)" ::: "memory"); SB(); } while (0)
#define CNT(x) ++x
#define STAMPA(i)
#define STAMPB(i) STAMP(i)

DEVI v4u tagged(v4f v, u32 tag) {
  return (v4u){__builtin_amdgcn_perm(fu(v.x), tag, 0x07060500u), __builtin_amdgcn_perm(fu(v.y), tag, 0x07060501u),
               __builtin_amdgcn_perm(fu(v.z), tag, 0x07060502u), __builtin_amdgcn_perm(fu(v.w), tag, 0x07060503u)};
}
DEVI u32 tagof(v4u d) {
  return __builtin_amdgcn_perm(__builtin_amdgcn_perm(d.w, d.z, 0x0c0c0400u), __builtin_amdgcn_perm(d.y, d.x, 0x0c0c0400u), 0x05040100u);
}
DEVI v4f untag(v4u d) { return (v4f){uf(d.x & ~255u), uf(d.y & ~255u), uf(d.z & ~255u), uf(d.w & ~255u)}; }
DEVI u32 sum4(v4u d) { return d.x + d.y + d.z + d.w; }
template <int T> DEVI v4u onehot(u32 v, const u32* msk) {
  return (v4u){dppu<0x150 + T>(v) & msk[0], dppu<0x150 + T>(v) & msk[1], dppu<0x150 + T>(v) & msk[2], dppu<0x150 + T>(v) & msk[3]};
}
template <int I, int N, class F> DEVI void sfor(F&& f) {
  if constexpr (I < N) { f(std::integral_constant<int, I>{}); sfor<I + 1, N>(f); }
}
DEVI void lnstat(v4u xa, u32 xb, float& mb, float& rs) {
  const float pv = uf(__builtin_amdgcn_readfirstlane(xa.x) << 16);
  float xv[10] = {bflo(xa.x), bfhi(xa.x), bflo(xa.y), bfhi(xa.y), bflo(xa.z), bfhi(xa.z), bflo(xa.w), bfhi(xa.w), bflo(xb), bfhi(xb)};
  float s1a = 0.f, s1b = 0.f, s2a = 0.f, s2b = 0.f;
#pragma unroll
  for (int i = 0; i < 5; ++i) { float d = xv[2 * i] - pv, e = xv[2 * i + 1] - pv; s1a += d; s1b += e; s2a += d * d; s2b += e * e; }
  float s = sw32add(s1a + s1b, s2a + s2b);
  s = sw16add(s, s);
  s = rowsum(s);
  const float m1 = uf(__builtin_amdgcn_readlane(fu(s), 0)) * (1.0f / 640.0f), m2 = uf(__builtin_amdgcn_readlane(fu(s), 32)) * (1.0f / 640.0f);
  const float var = fmaxf(m2 - m1 * m1, 0.f);
  mb = rb(pv + m1);
  rs = __builtin_amdgcn_rcpf(rb(__builtin_amdgcn_sqrtf(rb(rb(var) + 1e-5f))));
}

__global__ void __launch_bounds__(512, 2)
mla_decode_kernel(const void* __restrict__ px, const void* __restrict__ pckv, const void* __restrict__ pwq, const void* __restrict__ pwuk,
                  void* __restrict__ pws, const void* __restrict__ pwuv, const void* __restrict__ pwo, const void* __restrict__ pw1,
                  const void* __restrict__ pw2, void* __restrict__ pout) {
  __shared__ __attribute__((aligned(1024))) char smem[L_END];
  u64 pc;
  asm volatile("s_getpc_b64 %0" : "=s"(pc));
  asm volatile("" ::"v"((LAS char*)smem));
  u32 stamp[16];
#pragma unroll
  for (int i = 0; i < 16; ++i) stamp[i] = 0;
  u32 nsp2 = 0, nsp3 = 0, nsp5 = 0;
  const u32 clk0 = (u32)__builtin_amdgcn_s_memtime();
  STAMP(0);
  const u32 tid = threadIdx.x, lane = tid & 63, n = lane & 15, g = lane >> 4;
  const u32 w = __builtin_amdgcn_readfirstlane(tid >> 6);
  const u32 b = blockIdx.x, xcd = b & 7, r = b >> 3;
  if (CODE_BYTES && w == 7) dma16(mk((const void*)(pc & ~255ull), CODE_BYTES), lane * 16, r * 1024, r < 16 ? L_X + 2048 : L_PFD + 7 * 1024);
  asm volatile("" ::"s"(px), "s"(pckv), "s"(pwq), "s"(pwuk), "s"(pwuv), "s"(pwo), "s"(pw1), "s"(pw2), "s"(pout), "s"(pws));
  const rsrc_t rws = mk(pws, WS_BYTES);
  const rsrc_t rx = mk(px, 1280);
  u64 ep;
  auto ep_load = [&] { asm volatile("s_load_dwordx2 %0, %1, 0x0" : "=s"(ep) : "s"(pws) : "memory"); };
  u32 tlo, thi, tgh;
  auto ep_use = [&] {
    asm volatile("" : "+s"(ep));
    tlo = (u32)(ep + 1); thi = (u32)((ep + 1) >> 32); tgh = tlo * 0x9e3779b1u + thi * 0x85ebca6bu;
  };

  if (__builtin_expect(r < 16, 1)) {
    const u32 h = xcd * 4 + (r & 3), cq = r >> 2, wi = h * 4 + cq;
    const rsrc_t rwq = mk(pwq, 640u * 4096 * 2), rwuk = mk(pwuk, 512u * 4096 * 2);
    if (w == 0) { dma16(rx, lane * 16, 0, L_X); dma16(rx, lane * 16, 1024, L_X + 1024); }
    if (w == 1) __builtin_amdgcn_raw_buffer_store_b32(~0u, rws, lane * 4, WS_QA + cq * 8192 + h * 256, 16);
    SB();
    v4u wq[20], wuk[4];
    const u32 wq_v = (4 * w + g) * 8192 + h * 256 + n * 16, wuk_v = g * 8192 + ((n ^ g) << 4);
    auto fld = [&](auto ic) {
      constexpr int i = ic;
      if constexpr (i >= UP && i < UP + 4) {
        dma16(rwuk, wuk_v ^ ((i - UP) << 6), (cq * 128 + 16 * w + 4 * (i - UP)) * 8192 + h * 256, L_UK + w * 4096 + (i - UP) * 1024);
      } else {
        constexpr int t = i < UP ? i : i - 4;
        u32 tt = t + 5 * cq;
        tt = tt >= 20 ? tt - 20 : tt;
        wq[t] = ld16p(rwq, wq_v, tt * 262144);
      }
    };
    sfor<0, WD>(fld);
    SB();
    STAMP(1);
    ep_load();
    u32 msk[4];
#pragma unroll
    for (int q = 0; q < 4; ++q) { msk[q] = (q == (int)((n & 7) >> 1)) ? ((n & 1) ? 0xffff0000u : 0x0000ffffu) : 0u; PIN(msk[q]); }
    u32 a_qw = L_QP + w * 320 + n * 16 + g * 8, a_qr = L_QP + (n & 7) * 320 + g * 16, a_pub = WS_QA + cq * 8192 + h * 256 + (((2 * w + (n >> 3)) ^ sig(h & 15)) << 4) + (n & 7) * 2;
    u32 t0 = n + 5 * cq, t1 = 16 + (n & 3) + 5 * cq;
    t0 = t0 >= 20 ? t0 - 20 : t0; t1 = t1 >= 20 ? t1 - 20 : t1;
    u32 a_x = L_X + lane * 16, a_xb = L_X + 1024 + lane * 4, a_x0 = L_X + (32 * t0 + 4 * w + g) * 2, a_x1 = L_X + (32 * t1 + 4 * w + g) * 2;
    u32 a_uk = L_UK + w * 4096 + n * 256 + ((g ^ n) << 4);
    PIN(a_qw); PIN(a_qr); PIN(a_pub); PIN(a_x); PIN(a_xb); PIN(a_x0); PIN(a_x1); PIN(a_uk);
    SB();
    VMW(WD);
    bar();
    ep_use();
    if (w == 1 && lane == 0) st8((v2u){tlo, thi}, rws, 0, WS_FL + wi * 8);
    u32 x0, x1;
    {
      const v4u xa = lds_ld<v4u>(a_x);
      const u32 xb = lds_ld<u32>(a_xb), xg0 = lds_ld<unsigned short>(a_x0), xg1 = lds_ld<unsigned short>(a_x1);
      SB();
      float mb, rs;
      lnstat(xa, xb, mb, rs);
      const u32 dp = pk(bflo(xg0) - mb, bflo(xg1) - mb);
      const float a0 = bflo(dp) * rs, a1 = bfhi(dp) * rs;
      x0 = pk(a0, a0); x1 = pk(a1, a1);
    }
    PIN(x0); PIN(x1);
    SB();
    STAMP(4);
    v4f qa = {0.f, 0.f, 0.f, 0.f};
    sfor<0, 24>([&](auto ic) {
      constexpr int i = ic, t = i < UP ? i : i - 4;
      constexpr bool isq = i < UP || i >= UP + 4;
      v4u oh;
      if constexpr (isq) { oh = onehot<(t & 15)>(t < 16 ? x0 : x1, msk); PIN(oh); }
      VMW(i + WD <= 24 ? WD - 1 : 23 - i);
      if constexpr (i + WD < 24) fld(std::integral_constant<int, i + WD>{});
      if constexpr (isq) { PIN(wq[t]); qa = mfma(oh, wq[t], qa); }
      if constexpr (i == UP + 3) {
#pragma unroll
        for (int u = 0; u < 4; ++u) wuk[u] = lds_ld<v4u>(a_uk ^ (u << 6));
      }
      if constexpr (i == 0) STAMP(3);
      if constexpr (i + WD == 23) STAMP(5);
      SB();
    });
    if (g < 2) lds_st<v2u>(a_qw, (v2u){pk(qa.x, qa.y), pk(qa.z, qa.w)});
    STAMP(6);
    bar();
    {
      v4u qf[4];
#pragma unroll
      for (int u = 0; u < 4; ++u) qf[u] = lds_ld<v4u>(a_qr + u * 64);
      v4f qb = {0.f, 0.f, 0.f, 0.f};
#pragma unroll
      for (int u = 0; u < 4; ++u) qb = mfma(qf[u], wuk[u], qb);
      float v = (qb.x + qb.y) + (qb.z + qb.w);
      v = sw16add(v, v) * (0.08838834764831845f * 1.4426950408889634f);
      const u32 p = pk(v, dpp<0xb1>(v));
      if ((lane & 0x31) == 0) __builtin_amdgcn_raw_buffer_store_b32(min(p, ~1u), rws, a_pub, 0, 16);
    }
    SB();
    STAMP(7);
    const rsrc_t rwuv = mk(pwuv, 512u * 4096 * 2), rwo = mk(pwo, 4096u * 640 * 2);
    const u32 uv_v = g * 8192 + ((n ^ ((((w & 3) << 1) | (g & 1)) << 1)) << 4), wo_v = g * 1280 + ((n ^ (((w & 1) * 4 + g) << 1)) << 4);
    const u32 q4 = n >> 2, p4 = n & 3;
    u32 a_wo[5], a_uv = L_UV + (16 * q4 + g) * 256 + ((w ^ ((q4 << 1) | (g & 1))) << 5) + p4 * 8;
#pragma unroll
    for (int k = 0; k < 5; ++k) {
      const u32 rho = 5 * w + (k < 4 ? p4 : 4), i = k < 4 ? k : p4;
      const u32 c4 = (rho >> 1) * 8 + 2 * i + (rho & 1);
      a_wo[k] = L_WO + (g * 5 + (c4 >> 5)) * 1024 + q4 * 256 + ((((c4 >> 2) & 7) ^ ((g & 1) * 4 + q4)) << 5) + (c4 & 3) * 8;
      PIN(a_wo[k]);
    }
    u32 a_cw = L_CP + w * 320 + n * 16 + g * 8, a_cr = L_CP + (n & 7) * 320 + g * 16, a_mr = L_ML + (g & 1) * 16;
    u32 a_xw = L_CX + ((((w >> 1) - cq) & 3) * 32 + (n >> 2) * 8 + (w & 1) * 4 + (n & 3)) * 2, a_xr = L_CX + g * 16;
    u32 a_in = g * OSK + n * 16, a_so = (lane >> 2) * 16384 + (lane & 3) * 16, a_ml = (4 * (n & 3) + g) * 512;
    u32 a_cs = (4 * (n & 3) + g) * 2048 + (n >> 3) * 256 + (((h >> 2) & 3) * 16 + (h & 1) * 8 + (n & 4) + (((h >> 4) ^ (n >> 3)) & 1) * 2 + ((h >> 1) & 1)) * 4;
    PIN(a_uv); PIN(a_cw); PIN(a_cr); PIN(a_mr); PIN(a_xw); PIN(a_xr); PIN(a_in); PIN(a_so); PIN(a_ml); PIN(a_cs);
    v4u uv[4], wo[4][5];
    SB();
    if (BK_SLEEP) __builtin_amdgcn_s_sleep(BK_SLEEP);
    static_assert(9 + BD <= 19 && UVP % 5 == 0 && UVP >= 5 && UVP <= 15, "quarter 3 reuses the slot of quarter 0");
    sfor<0, 24 + BD>([&](auto ic) {
      constexpr int i = ic;
      if constexpr (i >= BD) VMW(i < 24 ? BD - 1 : 23 - (i - BD));
      if constexpr (i >= 24) {
      } else if constexpr (i >= UVP && i < UVP + 4) {
        dma16(rwuv, uv_v, (128 * (w & 3) + 32 * cq + 16 * (w >> 2) + 4 * (i - UVP)) * 8192 + h * 256, L_UV + (4 * w + i - UVP) * 1024);
      } else {
        constexpr int j = i < UVP ? i : i - 4, u = j / 5, mb = j % 5;
        dma16(rwo, wo_v, (h * 128 + ((u + cq) & 3) * 32 + 4 * w) * 1280 + mb * 256, L_WO + (((u % 3) * 8 + w) * 5 + mb) * 1024);
      }
      constexpr int d = i - BD, dj = d < UVP ? d : d - 4;
      if constexpr (i == 0) STAMPA(14);
      if constexpr (i == BD - 1) STAMPA(2);
      if constexpr (d == UVP + 3) {
        STAMPA(13);
        bar();
#pragma unroll
        for (int u = 0; u < 4; ++u) uv[u] = trd2(a_uv + (64 * (u >> 1) + 4 * (u & 1)) * 256, a_uv + (64 * (u >> 1) + 4 * (u & 1) + 8) * 256);
        SB();
      } else if constexpr (d >= 0 && !(d >= UVP && d < UVP + 4) && dj % 5 == 4) {
        constexpr int u = dj / 5;
        if constexpr (u < 3) STAMPA(10 + u);
        bar();
        if constexpr (u == 3) STAMP(8);
#pragma unroll
        for (int k = 0; k < 5; ++k) wo[u][k] = trd2(a_wo[k] + (u % 3) * 40960, a_wo[k] + (u % 3) * 40960 + 20480);
        SB();
      }
    });
#pragma unroll
    for (int u = 0; u < 4; ++u) {
      PIN(uv[u]);
#pragma unroll
      for (int k = 0; k < 5; ++k) PIN(wo[u][k]);
    }
    SB();
    STAMP(9);
    const u32 s_ml = WS_ML + w * 8192 + h * 16, s_cs = WS_CS + w * 32768 + cq * 512, s_in = WS_IN3 + h * OSH + cq * OSC + 16 * w * OSK;
    const u32 s_i4[4] = {s_in, s_in + 4 * OSK, s_in + 8 * OSK, s_in + 12 * OSK};
    v4u ml, pv[4], af[4];
    asm volatile("" : "=v"(ml));
    {
      u64 bad = 0x000f000f000f000full;
      u32 sp = 0;
      _Pragma("nounroll") do {
        if (ML_SLEEP) __builtin_amdgcn_s_sleep(ML_SLEEP);
        ldpoll1(ml, bad, rws, a_ml, s_ml);
        bad &= bal((ml.z != tlo) | (ml.w != thi));
      } while (bad && ++sp < SPIN_CAP);
    }
    STAMPB(2);
    if (P_SLEEP) __builtin_amdgcn_s_sleep(P_SLEEP);
#pragma unroll
    for (int i = 0; i < 4; ++i) pv[i] = ld16a(rws, a_in, s_i4[i]);
    u32 cv = ld4a(rws, a_cs, s_cs);
    SB();
    {
      float mw = mx(uf(ml.x), dpp<0xb1>(uf(ml.x)));
      mw = mx(mw, dpp<0x4e>(mw));
      { auto q = __builtin_amdgcn_permlane16_swap(fu(mw), fu(mw), false, false); mw = mx(uf(q[0]), uf(q[1])); }
      { auto q = __builtin_amdgcn_permlane32_swap(fu(mw), fu(mw), false, false); mw = mx(uf(q[0]), uf(q[1])); }
      const float e = __builtin_amdgcn_exp2f(uf(ml.x) - mw);
      const u32 ee = pk(e, e);
      float lw = bfhi(ee) * uf(ml.y);
      lw += dpp<0xb1>(lw); lw += dpp<0x4e>(lw);
      lw = sw16add(lw, lw); lw = sw32add(lw, lw);
      if (lane < 2) lds_st<float>(L_ML + w * 4 + lane * 32, lane ? lw : mw);
      af[0] = onehot<0>(ee, msk); af[1] = onehot<1>(ee, msk); af[2] = onehot<2>(ee, msk); af[3] = onehot<3>(ee, msk);
#pragma unroll
      for (int i = 0; i < 4; ++i) PIN(af[i]);
    }
    SB();
    v4f c;
    u64 bad;
    auto ochk = [&](auto first) {
      u32 t[4];
      c = (v4f){0.f, 0.f, 0.f, 0.f};
      sfor<0, 4>([&](auto ic) {
        constexpr int i = ic;
        if constexpr (first) VMW(4 - i);
        PIN(pv[i]);
        c = mfma(af[i], pv[i], c);
        t[i] = sum4(pv[i]);
        t[i] += dppu<0xb1>(t[i]);
        if constexpr (i & 1) { t[i] = (n & 1) ? t[i] : t[i - 1]; t[i] += dppu<0x4e>(t[i]); }
        if constexpr (first) SB();
      });
      if constexpr (first) VMW(0);
      PIN(cv);
      bad = bal(((n & 2) ? t[3] : t[1]) + tgh != cv);
    };
    ochk(std::true_type{});
    if (__builtin_expect(bad != 0, 0)) {
      u32 sp = 0;
      _Pragma("nounroll") do {
        u64 bo[4];
#pragma unroll
        for (int i = 0; i < 4; ++i) bo[i] = ((bad >> i) & 0x1111111111111111ull) * 15;
        ldpoll(pv, cv, bo, bad, rws, a_in, s_i4, a_cs, s_cs);
        ochk(std::false_type{});
        CNT(nsp3);
      } while (bad && ++sp < SPIN_CAP);
    }
    STAMPB(10);
    if (g < 2) lds_st<v2u>(a_cw, (v2u){pk(c.x, c.y), pk(c.z, c.w)});
    STAMPB(11);
    bar();
    {
      v4u cf[4];
#pragma unroll
      for (int u = 0; u < 4; ++u) cf[u] = lds_ld<v4u>(a_cr + u * 64);
      const v4f m4 = lds_ld<v4f>(a_mr), l4 = lds_ld<v4f>(a_mr + 32);
      v4f cx = {0.f, 0.f, 0.f, 0.f};
#pragma unroll
      for (int u = 0; u < 4; ++u) cx = mfma(cf[u], uv[u], cx);
      const float mg = sw16max(fmaxf(fmaxf(m4.x, m4.y), fmaxf(m4.z, m4.w)));
      const float e0 = __builtin_amdgcn_exp2f(m4.x - mg), e1 = __builtin_amdgcn_exp2f(m4.y - mg);
      const float e2 = __builtin_amdgcn_exp2f(m4.z - mg), e3 = __builtin_amdgcn_exp2f(m4.w - mg);
      float den = (e0 * l4.x + e1 * l4.y) + (e2 * l4.z + e3 * l4.w);
      const float inv = __builtin_amdgcn_rcpf(sw16add(den, den));
      float v = (e0 * cx.x + e1 * cx.y) + (e2 * cx.z + e3 * cx.w);
      v = sw16add(v, v) * inv;
      if (g == 0) lds_st<unsigned short>(a_xw, (unsigned short)pk(v, v));
    }
    STAMPB(12);
    bar();
    {
      v4u xf[4];
#pragma unroll
      for (int u = 0; u < 4; ++u) xf[u] = lds_ld<v4u>(a_xr + u * 64);
      v4f s[5];
#pragma unroll
      for (int k = 0; k < 5; ++k) s[k] = (v4f){0.f, 0.f, 0.f, 0.f};
#pragma unroll
      for (int u = 0; u < 4; ++u)
#pragma unroll
        for (int k = 0; k < 5; ++k) s[k] = mfma(xf[u], wo[u][k], s[k]);
      const u32 s4 = fu(s[4].x);
      const v4f d = {uf(__builtin_amdgcn_update_dpp(fu(s[0].x), s4, 0xe4, 0x2, 0xf, false)), uf(__builtin_amdgcn_update_dpp(fu(s[1].x), s4, 0x104, 0x2, 0xf, false)),
                     uf(__builtin_amdgcn_update_dpp(fu(s[2].x), s4, 0x108, 0x2, 0xf, false)), uf(__builtin_amdgcn_update_dpp(fu(s[3].x), s4, 0x10c, 0x2, 0xf, false))};
      if (lane < 20) st16(tagged(d, tlo), rws, a_so, WS_IN5 + (5 * w * 256 + wi) * 64);
    }
    STAMPB(13);
  } else {
    const u32 ks = KSW & 1 ? xcd * 16 + r - 16 : b - 128, kc = KSW & 2 ? xcd * 16 + r - 16 : b - 128;
    const rsrc_t rck = mk(pckv, 16384u * 1024);
    u32 a_o = (4 * g + (n >> 3)) * OSH + (n & 7) * 16;
    const u32 s_o = WS_IN3 + (w >> 1) * OSC + ks * OSK + (w & 1) * 128;
    if (w == 0) { dma16(rx, lane * 16, 0, L_XK); dma16(rx, lane * 16, 1024, L_XK + 1024); }
    const u32 fb = xcd + 8 * ((r >> 2) & 3), yi = fb * 4 + (r & 3);
    float xs[4];
    const rsrc_t rwuk = mk(pwuk, 512u * 4096 * 2);
    const u32 ck_v = g * 1024 + ((n ^ (g << 2)) << 4), ck_s = (kc * 128 + 16 * w) * 1024;
    u32 ra[4], rq[4], va[2], a_pr[4], a_mx, a_pw, a_f1, a_f2;
    v4u kf[16], vt[4][4];
    auto stage = [&](auto sc) {
      constexpr int s = sc;
      bar();
#pragma unroll
      for (int u = 0; u < 4; ++u) kf[4 * s + u] = lds_ld<v4u>(L_CKV + ra[u] + w * 4096 + s * 32768);
#pragma unroll
      for (int kk = 0; kk < 4; ++kk) vt[kk][s] = trd2(va[0] + s * 32768 + kk * 8192, va[1] + s * 32768 + kk * 8192);
      if constexpr (s == 0) {
        ep_use();
        const v4u xa = lds_ld<v4u>(L_XK + lane * 16);
        const u32 xb = lds_ld<u32>(L_XK + 1024 + lane * 4);
        float mb, rs;
        lnstat(xa, xb, mb, rs);
#pragma unroll
        for (int i = 0; i < 4; ++i) {
          xs[i] = rb(rb(bflo(lds_ld<unsigned short>(L_XK + w * 256 + i * 64 + (lane & 62))) - mb) * rs);
          PIN(xs[i]);
        }
      }
      SB();
    };
    auto ckld = [&](int s, int j) { dma16s(rck, ck_v ^ (j << 4), ck_s + j * 4096 + s * 256, L_CKV + s * 32768 + (16 * w + 4 * j) * 256); };
    static_assert(KD >= 5 && KD <= 8 && QW < KD, "");
    constexpr int NA = PF + 17, IF = PF + FPOS;
    static_assert(IF < NA - QW && FPOS > 8, "");
    v2u fv;
    sfor<0, NA>([&](auto ic) {
      constexpr int i = ic;
      if constexpr (i >= KD) VMW(KD - 1);
      if constexpr (i == PF && KS) __builtin_amdgcn_s_sleep(KS);
      if constexpr (i < PF) dma16(rwuk, lane * 16, ((r - 16) * 32 + 4 * w + i) * 8192 + xcd * 1024, L_PFD + w * 1024);
      else if constexpr (i == IF) fv = ld8a(rws, n * 8, WS_FL + w * 128);
      else { constexpr int k = i - PF - (i > IF); ckld(k >> 2, k & 3); }
      if constexpr (i == KD - 1) {
        SB();
        STAMP(1);
        ep_load();
        const u32 q = n >> 2, p = n & 3;
#pragma unroll
        for (int u = 0; u < 4; ++u) { ra[u] = n * 256 + (((g * 4 + u) ^ sig(n)) << 4); PIN(ra[u]); }
#pragma unroll
        for (int t = 0; t < 2; ++t) {
          const u32 row = 8 * g + 4 * t + q;
          va[t] = L_CKV + row * 256 + (((2 * w + (p >> 1)) ^ sig(row & 15)) << 4) + 8 * (p & 1);
          PIN(va[t]);
        }
        a_mx = L_MX + n * 4; a_pw = L_P + n * 256 + (((2 * w + (g >> 1)) ^ n) << 4) + (g & 1) * 8;
#pragma unroll
        for (int kk = 0; kk < 4; ++kk) { a_pr[kk] = L_P + (w & 1) * 4096 + n * 256 + (((4 * kk + g) ^ n) << 4); PIN(a_pr[kk]); }
#pragma unroll
        for (int u = 0; u < 4; ++u) { rq[u] = ra[u] + L_QA; PIN(rq[u]); }
        a_f1 = (lane >> 1) * 3456 + (lane & 1) * 16; a_f2 = w < 4 ? (lane >> 3) * 3456 + (lane & 7) * 16 : lane * 16;
        PIN(a_mx); PIN(a_pw); PIN(a_o); PIN(a_f1); PIN(a_f2);
        SB();
      }
      if constexpr (i - KD == PF + 3) stage(std::integral_constant<int, 0>{});
      if constexpr (i - KD == PF + 7) stage(std::integral_constant<int, 1>{});
    });
    const rsrc_t rf = mk(w < 4 ? pw1 : pw2, fb < 27 && (FFP == 0 || w >= 4) ? 640u * 1728 * 2 : 0u);
    const u32 ff_s = w < 4 ? (160 * (r & 3) + 40 * w) * 3456 + fb * 128 : yi * 20480 + (w - 4) * 5120, ff_d = w < 4 ? 8 * 3456 : 1024;
    auto ffn_ld = [&](int i) { dma16(rf, a_f2, ff_s + i * ff_d, L_FF + w * 5120 + i * 1024); };
    if (w == 0 && lane < 32) lds_st<float>(L_MX + lane * 4, -__builtin_inff());
    u32 a_q = lane * 16, a_qs = L_QA + w * 1024 + lane * 16;
    u32 a_lw = L_L + ((g >> 1) * 16 + n) * 48 + w * 4, a_tl = L_L + ((w & 1) * 16 + n) * 48, a_ts = WS_CS + ks * 2048 + w * 256 + lane * 4;
    u32 a_ms = WS_ML + ks * 512 + (w & 1) * 256 + n * 16;
    PIN(a_q); PIN(a_qs); PIN(a_tl); PIN(a_ts); PIN(a_lw); PIN(a_ms);
    SB();
    if constexpr (QW < 4) { VMW(FPOS > 12 ? 5 : 4); stage(std::integral_constant<int, 2>{}); }
    VMW(QW);
    if (QS) __builtin_amdgcn_s_sleep(QS);
    v4u qr[4];
#pragma unroll
    for (int j = 0; j < 4; ++j) qr[j] = ld16a(rws, a_q, WS_QA + j * 8192 + w * 1024);
    STAMP(2);
    PIN(fv);
    u64 bf = bal((fv.x != tlo) | (fv.y != thi));
    if constexpr (QW >= 4) { VMW(8); stage(std::integral_constant<int, 2>{}); }
    if constexpr (S3B) { VMW(4); stage(std::integral_constant<int, 3>{}); }
    STAMP(3);
    u64 bq[4];
    auto qchk = [&](int j) {
      PIN(qr[j]);
      lds_st<v4u>(a_qs + j * 8192, qr[j]);
      bq[j] = bal(max(max(qr[j].x, qr[j].y), max(qr[j].z, qr[j].w)) == ~0u);
    };
    sfor<0, 4>([&](auto jc) { VMW(3 - jc); qchk(jc); SB(); });
    STAMP(4);
    if (__builtin_expect((bf | bq[0] | bq[1] | bq[2] | bq[3]) != 0, 0)) {
      const u32 s_q[4] = {WS_QA + w * 1024, WS_QA + 8192 + w * 1024, WS_QA + 16384 + w * 1024, WS_QA + 24576 + w * 1024};
      u32 sp = 0;
      if (bf) {
        _Pragma("nounroll") do {
          asm volatile("" ::: "memory");
          fv = __builtin_amdgcn_raw_buffer_load_b64(rws, n * 8, WS_FL + w * 128, 16);
        } while (bal((fv.x != tlo) | (fv.y != thi)) && ++sp < SPIN_CAP);
        bq[0] = bq[1] = bq[2] = bq[3] = ~0ull;
      }
      _Pragma("nounroll") do {
        if (QRS) __builtin_amdgcn_s_sleep(QRS);
        ldpoll4(qr, bq, rws, a_q, s_q);
#pragma unroll
        for (int j = 0; j < 4; ++j) qchk(j);
        CNT(nsp2);
      } while ((bq[0] | bq[1] | bq[2] | bq[3]) && ++sp < SPIN_CAP);
    }
    bar();
    STAMP(5);
    v4f sc[2] = {{0.f, 0.f, 0.f, 0.f}, {0.f, 0.f, 0.f, 0.f}};
    {
      v4u qf[32];
      auto qrd = [&](auto jc) { constexpr int j = jc, u = j >> 1; qf[j] = lds_ld<v4u>(rq[u & 3] + (j & 1) * 4096 + (u >> 2) * 8192); };
      sfor<0, SD>(qrd);
      SB();
      sfor<0, 32>([&](auto jc) {
        constexpr int j = jc;
        sc[j & 1] = mfma(kf[j >> 1], qf[j], sc[j & 1]);
        if constexpr (j + SD < 32) qrd(std::integral_constant<int, j + SD>{});
        if constexpr (FFP < 2 && j % FSP == 0 && j / FSP >= 1 && j / FSP <= 5) ffn_ld(j / FSP - 1);
        if constexpr (!S3B && j + SD == 32) {
#pragma unroll
          for (int u = 0; u < 4; ++u) kf[12 + u] = lds_ld<v4u>(L_CKV + ra[u] + w * 4096 + 3 * 32768);
        }
        SB();
      });
    }
    STAMP(6);
#pragma unroll
    for (int ht = 0; ht < 2; ++ht)
      __hip_atomic_fetch_max((LAS float*)(a_mx + ht * 64), mx(mx(sc[ht].x, sc[ht].y), mx(sc[ht].z, sc[ht].w)), __ATOMIC_RELAXED,
                             __HIP_MEMORY_SCOPE_WORKGROUP);
    STAMP(7);
    bar();
    float mrow[2], ls[2];
#pragma unroll
    for (int ht = 0; ht < 2; ++ht) mrow[ht] = lds_ld<float>(a_mx + ht * 64);
    if constexpr (!S3B) {
#pragma unroll
      for (int kk = 0; kk < 4; ++kk) vt[kk][3] = trd2(va[0] + 3 * 32768 + kk * 8192, va[1] + 3 * 32768 + kk * 8192);
    }
    SB();
#pragma unroll
    for (int ht = 0; ht < 2; ++ht) {
      const float p0 = __builtin_amdgcn_exp2f(sc[ht].x - mrow[ht]), p1 = __builtin_amdgcn_exp2f(sc[ht].y - mrow[ht]);
      const float p2 = __builtin_amdgcn_exp2f(sc[ht].z - mrow[ht]), p3 = __builtin_amdgcn_exp2f(sc[ht].w - mrow[ht]);
      lds_st<v2u>(a_pw + ht * 4096, (v2u){pk(p0, p1), pk(p2, p3)});
      ls[ht] = (p0 + p1) + (p2 + p3);
    }
    {
      const float x = sw32add(ls[0], ls[1]);
      lds_st<float>(a_lw, sw16add(x, x));
    }
    STAMP(8);
    if (w >= 4) __builtin_amdgcn_s_setprio(1);
    bar();
    v4u pf[2][4];
#pragma unroll
    for (int ht = 0; ht < 2; ++ht)
#pragma unroll
      for (int kk = 0; kk < 4; ++kk) pf[ht][kk] = lds_ld<v4u>(a_pr[kk] ^ (ht * 4096));
    u32 cs[2];
    auto mlst = [&] {
      if ((w >> 1) == MLW) {
        const v4f l4 = lds_ld<v4f>(a_tl) + lds_ld<v4f>(a_tl + 16);
        if (g == 0) st16((v4u){fu((w & 1) ? mrow[1] : mrow[0]), fu((l4.x + l4.y) + (l4.z + l4.w)), tlo, thi}, rws, a_ms, 0);
      }
    };
    if (MLP == 0) mlst();
    SB();
    STAMPA(10);
#pragma unroll
    for (int ht = 0; ht < 2; ++ht) {
      v4f o[4];
#pragma unroll
      for (int ct = 0; ct < 4; ++ct) o[ct] = (v4f){0.f, 0.f, 0.f, 0.f};
#pragma unroll
      for (int kk = 0; kk < 4; ++kk)
#pragma unroll
        for (int ct = 0; ct < 4; ++ct) {
          o[ct] = mfma(pf[ht][kk], vt[kk][ct], o[ct]);
        }
      if (MLP == 1 + 2 * ht) mlst();
      STAMPA(11 + 2 * ht);
#pragma unroll
      for (int pr = 0; pr < 2; ++pr) {
        const u32 x0 = pk(o[0][2 * pr], o[1][2 * pr]), x1 = pk(o[2][2 * pr], o[3][2 * pr]);
        const u32 y0 = pk(o[0][2 * pr + 1], o[1][2 * pr + 1]), y1 = pk(o[2][2 * pr + 1], o[3][2 * pr + 1]);
        const v4u d = {(u32)__builtin_amdgcn_update_dpp(x0, y0, 0x128, 0xf, 0xc, false), (u32)__builtin_amdgcn_update_dpp(x1, y1, 0x128, 0xf, 0xc, false),
                       (u32)__builtin_amdgcn_update_dpp(y0, x0, 0x128, 0xf, 0x3, false), (u32)__builtin_amdgcn_update_dpp(y1, x1, 0x128, 0xf, 0x3, false)};
        st16(d, rws, a_o, s_o + (16 * (ht ^ (w & 1)) + 2 * pr) * OSH);
        u32 t = sum4(d);
        t += dppu<0xb1>(t);
        cs[ht] = pr && !(n & 1) ? cs[ht] : t;
      }
      cs[ht] += dppu<0x4e>(cs[ht]);
      if (MLP == 2 + 2 * ht) mlst();
      STAMPA(12 + 2 * ht);
      if (PVS) SB();
    }
    __builtin_amdgcn_raw_buffer_store_b32(((n & 2) ? cs[1] : cs[0]) + tgh, rws, a_ts, 0, 16);
    STAMP(9);
    if (fb < 27) {
      if (FBAR) __builtin_amdgcn_s_barrier();
      if (FFP == 2 && w >= 4) {
#pragma unroll
        for (int i = 0; i < 5; ++i) ffn_ld(i);
      }
      if (w >= 5) VMW(FFP == 2 ? 0 : 5);
      else {
        v4u w1r[4], w2r[4];
        const rsrc_t rw1 = mk(pw1, 640u * 1728 * 2);
#pragma unroll
        for (int i = 0; i < 4; ++i) w1r[i] = ld16p(rw1, a_f1, (128 * w + 32 * i) * 3456 + yi * 32);
        VMW(0);
#pragma unroll
        for (int i = 0; i < 4; ++i) PIN(w1r[i]);
        STAMPB(10);
        float ha[8];
#pragma unroll
        for (int e = 0; e < 8; ++e) ha[e] = 0.f;
#pragma unroll
        for (int i = 0; i < 4; ++i) {
#pragma unroll
          for (int q = 0; q < 4; ++q) { ha[2 * q] += xs[i] * bflo(w1r[i][q]); ha[2 * q + 1] += xs[i] * bfhi(w1r[i][q]); }
        }
        float h0 = sw16add(sw32add(ha[0], ha[2]), sw32add(ha[1], ha[3]));
        float h1 = sw16add(sw32add(ha[4], ha[6]), sw32add(ha[5], ha[7]));
        h0 += dpp<0x128>(h0); h1 += dpp<0x128>(h1); h0 += dpp<0x124>(h0); h1 += dpp<0x124>(h1); h0 += dpp<0x122>(h0); h1 += dpp<0x122>(h1);
        if (n < 4) lds_st<float>(L_QA + w * 64 + ((n & 1) * 8 + (n >> 1) * 4 + g) * 4, n < 2 ? h0 : h1);
        STAMPB(11);
        bar();
#pragma unroll
        for (int i = 0; i < 4; ++i) w2r[i] = lds_ld<v4u>(L_FF + 20480 + (4 * i + g) * 1280 + w * 256 + n * 16);
        float hs = 0.f;
#pragma unroll
        for (int v = 0; v < 5; ++v) hs += lds_ld<float>(L_QA + v * 64 + (4 * (n & 3) + g) * 4);
        hs = fmaxf(hs, 0.f);
        const u32 hh = pk(hs, hs);
        u32 msk[4];
#pragma unroll
        for (int q = 0; q < 4; ++q) msk[q] = (q == (int)((n & 7) >> 1)) ? ((n & 1) ? 0xffff0000u : 0x0000ffffu) : 0u;
        v4f y = mfma(onehot<0>(hh, msk), w2r[0], (v4f){0.f, 0.f, 0.f, 0.f});
        y = mfma(onehot<1>(hh, msk), w2r[1], y);
        y = mfma(onehot<2>(hh, msk), w2r[2], y);
        y = mfma(onehot<3>(hh, msk), w2r[3], y);
        if (g < 2) st16(tagged(y, tlo), rws, ((n >> 2) * 2 + g) * 16384 + (n & 3) * 16, WS_IN5 + (w * 8 * 256 + 128 + yi) * 64);
        STAMPB(12);
      }
    } else if (w < 2) {
      const u32 rho = (yi - 108) * 2 + w;
      const u32 kg = (g & 1) * 2 + (g >> 1);
      const u32 col = (rho >> 1) * 32 + 8 * kg + 4 * (rho & 1) + (n & 3);
      const u32 xo = __builtin_amdgcn_raw_buffer_load_b16(rx, col * 2, 0, 0);
      float gt;
      {
        v4u yv[7];
        __builtin_amdgcn_s_sleep(Y_SLEEP);
        _Pragma("nounroll") for (u32 sp = 0;; ++sp) {
          asm volatile("" ::: "memory");
#pragma unroll
          for (int i = 0; i < 7; ++i) yv[i] = ld16<16>(rws, (i == 6 && lane >= 48 ? lane - 48 : lane) * 16 + (i & 3) * 1024, WS_IN5 + rho * 16384 + 8192 + (i >> 2) * 4096);
          bool ok = true;
#pragma unroll
          for (int i = 0; i < 7; ++i) ok &= tagof(yv[i]) == tlo;
          if (__all(ok) || sp >= SPIN_CAP) break;
          __builtin_amdgcn_s_sleep(4);
        }
        v4f a = lane < 48 ? untag(yv[6]) : (v4f){0.f, 0.f, 0.f, 0.f};
#pragma unroll
        for (int i = 0; i < 6; ++i) a += untag(yv[i]);
        float y = sw16add(sw32add(a.x, a.y), sw32add(a.z, a.w));
        y += dpp<0x128>(y); y += dpp<0x124>(y);
        const u32 n3 = n & 3;
        y = uf(__builtin_amdgcn_ds_bpermute((int)(((((n3 & 1) * 2 + (n3 >> 1)) << 4) + kg) * 4), fu(y)));
        y = rb(y);
        gt = rb(y / (1.0f + __expf(-y)));
      }
      float xf = bflo(xo);
      PIN(gt); PIN(xf);
      STAMPB(13);
      __builtin_amdgcn_s_sleep(RED_SLEEP);
      v4u sv[8];
      _Pragma("nounroll") for (u32 sp = 0;; ++sp) {
        asm volatile("" ::: "memory");
#pragma unroll
        for (int i = 0; i < 8; ++i) sv[i] = ld16<16>(rws, lane * 16 + (i & 3) * 1024, WS_IN5 + rho * 16384 + (i >> 2) * 4096);
        bool ok = true;
#pragma unroll
        for (int i = 0; i < 8; ++i) ok &= tagof(sv[i]) == tlo;
        if (__all(ok) || sp >= SPIN_CAP) break;
        CNT(nsp5);
      }
      STAMPB(14);
      v4f a = untag(sv[0]);
#pragma unroll
      for (int i = 1; i < 8; ++i) a += untag(sv[i]);
      float s = sw16add(sw32add(a.x, a.y), sw32add(a.z, a.w));
      s += dpp<0x128>(s); s += dpp<0x124>(s);
      const u32 o = pk(xf + rb(rb(s) * gt), 0.f);
      if (n < 4) ((unsigned short*)pout)[col] = (unsigned short)o;
      if (rho == 0 && lane == 0) __builtin_amdgcn_raw_buffer_store_b64((v2u){tlo, thi}, rws, 0, 0, 16);
    } else {
      VMW(5);
    }
  }
  STAMP(15);
  if (lane == 0) {
    u32* o = (u32*)((char*)pws + WS_DBG) + (b * 8 + w) * 32;
#pragma unroll
    for (int i = 0; i < 16; ++i) o[i] = stamp[i];
    o[19] = (u32)__builtin_amdgcn_s_memtime() - clk0;
    o[16] = nsp2; o[17] = nsp3; o[18] = nsp5;
  }
}

__global__ void kpre(unsigned* ts) { if (threadIdx.x == 0) ts[2048 * 32] = (unsigned)__builtin_amdgcn_s_memrealtime(); }
__global__ void kspin(const unsigned* ts) {
  unsigned ts0 = (unsigned)__builtin_amdgcn_s_memrealtime();
  unsigned t0 = ts[0]; int mn = 0, mx = -100000000;
  for (int i = threadIdx.x; i < 2048; i += 64) { int a = (int)(ts[i * 32] - t0); mn = a < mn ? a : mn; unsigned e = ts[i * 32 + 15]; if (e) { int d = (int)(e - t0); mx = d > mx ? d : mx; } }
  for (int o = 32; o; o >>= 1) { int a = __shfl_xor(mn, o), e = __shfl_xor(mx, o); mn = a < mn ? a : mn; mx = e > mx ? e : mx; }
  int pre = (int)(ts[2048 * 32] - t0);
  unsigned long long s = __builtin_amdgcn_s_memrealtime();
  long long d = MODE == 1 ? (mn - pre) : MODE == 2 ? ((int)(ts0 - t0) - mx) : MODE == 3 ? (int)((unsigned)s - ts0) : 0;
  d *= 20; if (d > 100000) d = 100000; if (d < 0) d = 0;
  while ((long long)(__builtin_amdgcn_s_memrealtime() - s) < d) __builtin_amdgcn_s_sleep(2);
}

extern "C" void kernel_launch(void* const* d_in, const int* in_sizes, int n_in, void* d_out, int out_size, void* d_ws, size_t ws_size,
                              hipStream_t stream) {
  kpre<<<1, 64, 0, stream>>>((unsigned*)((char*)d_ws + WS_DBG));
  mla_decode_kernel<<<256, 512, 0, stream>>>(d_in[0], d_in[1], d_in[2], d_in[3], d_ws, d_in[4], d_in[5], d_in[7], d_in[8], d_out);
  kspin<<<1, 64, 0, stream>>>((const unsigned*)((char*)d_ws + WS_DBG));
}
```

```cpp
#include <hip/hip_runtime.h>
#include <type_traits>

typedef unsigned u32;
typedef unsigned long long u64;
typedef u32 v4u __attribute__((ext_vector_type(4)));
typedef u32 v2u __attribute__((ext_vector_type(2)));
typedef float v4f __attribute__((ext_vector_type(4)));
typedef float v2f __attribute__((ext_vector_type(2)));
typedef __bf16 bfx8 __attribute__((ext_vector_type(8)));
typedef __bf16 bfx2 __attribute__((ext_vector_type(2)));
typedef short v4s __attribute__((ext_vector_type(4)));
typedef __amdgpu_buffer_rsrc_t rsrc_t;
#define LAS __attribute__((address_space(3)))
#define DEVI __device__ __forceinline__

#define WD 6
#define UP 10
#define FFP 0
#define UVP 5
#define BD 8
#define CODE_BYTES 25088
#define WPF 1
#define WPE 0
#define PG 15
#define PGJ 2
#define FSL 0
#define VTL 1
#define KD 16
#define ORD 5
#define QB 2
#define QM 5
#define FB 0
#define HHS 0
#define QRS 0
#define OSH 1024
#define OSC 256
#define OSK 33024

#define MLP 1
#define MLW 0
#define QS 0
#define PVS 1
#define FBAR 1
#define KS 4
#define SD 8
#define PF 8
#define BK_SLEEP 32
#define RED_SLEEP 64
#define BKT 430
#define TSB QM
#define DML 225
#define DO 255
#define DY 375
#define DS 410
#define TS_SLEEP 8
#define FFA 0
#define FFC 1
#define RP_SLEEP 0
#define Y_SLEEP 48
#define YP_SLEEP 4
#define ML_SLEEP 4
#define P_SLEEP 0
#define MLE 1
#define AUX_CK0 ""
#define AUX_CK1 " nt"

constexpr u32 WS_FL = 2048, WS_QA = 4096, WS_TS = 65536, WS_ML = 131072, WS_CS = 262144, WS_IN3 = 1u << 20, WS_IN5 = 6u << 20, WS_BYTES = 8u << 20;
constexpr u32 WS_DBG = 16u << 20;
constexpr u32 L_END = 163840;
constexpr u32 L_CKV = 0, L_QA = 131072, L_P = L_QA + 16384, L_MX = L_P + 8192, L_L = L_MX + 256, L_G = L_MX + 2048, L_FF = 0, L_XK = L_QA, L_PFD = L_QA + 8192;
constexpr u32 L_X = 0, L_UK = 4096, L_WO = 0, L_UV = 122880, L_QP = 155648, L_CP = L_UV, L_ML = L_UV + 4096, L_CX = L_UV + 4352;
constexpr u32 SPIN_CAP = 100000;

DEVI rsrc_t mk(const void* p, u32 bytes) { return __builtin_amdgcn_make_buffer_rsrc(const_cast<void*>(p), 0, bytes, 0x00020000); }
template <int AUX> DEVI v4u ld16(rsrc_t r, u32 v, u32 s) { return __builtin_amdgcn_raw_buffer_load_b128(r, v, s, AUX); }
DEVI void st16(v4u d, rsrc_t r, u32 v, u32 s) {
  __builtin_amdgcn_raw_buffer_store_b128(d, r, v, s, 16);
  asm volatile("s_nop 1" ::"v"(d));
}
DEVI float bflo(u32 x) { return __builtin_bit_cast(float, x << 16); }
DEVI float bfhi(u32 x) { return __builtin_bit_cast(float, x & 0xffff0000u); }
DEVI u32 pk(float a, float b) { v2f t = {a, b}; return __builtin_bit_cast(u32, __builtin_convertvector(t, bfx2)); }
DEVI float rb(float a) { return bfhi(pk(a, a)); }
DEVI u32 fu(float a) { return __builtin_bit_cast(u32, a); }
DEVI float uf(u32 a) { return __builtin_bit_cast(float, a); }
template <int C> DEVI float dpp(float x) { return uf(__builtin_amdgcn_update_dpp(0, fu(x), C, 0xf, 0xf, true)); }
template <int C> DEVI u32 dppu(u32 x) { return __builtin_amdgcn_update_dpp(0, x, C, 0xf, 0xf, true); }
DEVI float rowsum(float x) { x += dpp<0x128>(x); x += dpp<0x124>(x); x += dpp<0x122>(x); x += dpp<0x121>(x); return x; }
DEVI float sw32add(float a, float b) { auto p = __builtin_amdgcn_permlane32_swap(fu(a), fu(b), false, false); return uf(p[0]) + uf(p[1]); }
DEVI float sw16add(float a, float b) { auto p = __builtin_amdgcn_permlane16_swap(fu(a), fu(b), false, false); return uf(p[0]) + uf(p[1]); }
DEVI float sw32max(float a) { auto p = __builtin_amdgcn_permlane32_swap(fu(a), fu(a), false, false); return fmaxf(uf(p[0]), uf(p[1])); }
DEVI float sw16max(float a) { auto p = __builtin_amdgcn_permlane16_swap(fu(a), fu(a), false, false); return fmaxf(uf(p[0]), uf(p[1])); }
DEVI v4f mfma(v4u a, v4u b, v4f c) { return __builtin_amdgcn_mfma_f32_16x16x32_bf16(__builtin_bit_cast(bfx8, a), __builtin_bit_cast(bfx8, b), c, 0, 0, 0); }
DEVI float dot2(u32 a, u32 b, float c) { return __builtin_amdgcn_fdot2_f32_bf16(__builtin_bit_cast(bfx2, a), __builtin_bit_cast(bfx2, b), c, false); }
DEVI u32 sig(u32 r) { return ((r & 3) << 2) | ((r >> 2) & 3); }
template <class T> DEVI T lds_ld(u32 a) { return *(LAS T*)a; }
template <class T> DEVI void lds_st(u32 a, T v) { *(LAS T*)a = v; }
DEVI v2u trd(u32 a) { return __builtin_bit_cast(v2u, __builtin_amdgcn_ds_read_tr16_b64_v4i16((LAS v4s*)a)); }
DEVI v4u trd2(u32 a, u32 b) { v2u x = trd(a), y = trd(b); return (v4u){x.x, x.y, y.x, y.y}; }
DEVI void bar() { asm volatile("s_waitcnt lgkmcnt(0)\n\ts_barrier" ::: "memory"); }
DEVI void dma16(rsrc_t r, u32 voff, u32 soff, u32 ldsdst) {
  asm volatile("s_mov_b32 m0, %3\n\ts_nop 0\n\tbuffer_load_dwordx4 %0, %1, %2 offen lds" ::"v"(voff), "s"(r), "s"(soff), "s"(ldsdst) : "memory");
}
template <int T> DEVI void dma16s(rsrc_t r, u32 voff, u32 soff, u32 ldsdst) {
  if constexpr (T) asm volatile("s_mov_b32 m0, %3\n\ts_nop 0\n\tbuffer_load_dwordx4 %0, %1, %2 offen" AUX_CK1 " lds" ::"v"(voff), "s"(r), "s"(soff), "s"(ldsdst) : "memory");
  else asm volatile("s_mov_b32 m0, %3\n\ts_nop 0\n\tbuffer_load_dwordx4 %0, %1, %2 offen" AUX_CK0 " lds" ::"v"(voff), "s"(r), "s"(soff), "s"(ldsdst) : "memory");
}
DEVI void st8(v2u d, rsrc_t r, u32 v, u32 s) { __builtin_amdgcn_raw_buffer_store_b64(d, r, v, s, 16); }
DEVI v4u ld16a(rsrc_t r, u32 voff, u32 soff) {
  v4u d;
  asm volatile("buffer_load_dwordx4 %0, %1, %2, %3 offen sc1" : "=v"(d) : "v"(voff), "s"(r), "s"(soff) : "memory");
  return d;
}
DEVI v4u ld16p(rsrc_t r, u32 voff, u32 soff) {
  v4u d;
  asm volatile("buffer_load_dwordx4 %0, %1, %2, %3 offen" : "=v"(d) : "v"(voff), "s"(r), "s"(soff) : "memory");
  return d;
}
DEVI v2u ld8a(rsrc_t r, u32 voff, u32 soff) {
  v2u d;
  asm volatile("buffer_load_dwordx2 %0, %1, %2, %3 offen sc1" : "=v"(d) : "v"(voff), "s"(r), "s"(soff) : "memory");
  return d;
}
DEVI u32 ld4a(rsrc_t r, u32 voff, u32 soff) {
  u32 d;
  asm volatile("buffer_load_dword %0, %1, %2, %3 offen sc1" : "=v"(d) : "v"(voff), "s"(r), "s"(soff) : "memory");
  return d;
}
DEVI void ldpoll(v4u* d, u32& t, u64 m0, u64 m1, u64 mt, rsrc_t r, u32 vd, u32 s0, u32 s1, u32 vt, u32 st) {
  asm volatile("s_mov_b64 exec, %9\n\tbuffer_load_dwordx4 %0, %3, %5, %6 offen sc1\n\t"
               "s_mov_b64 exec, %10\n\tbuffer_load_dwordx4 %1, %3, %5, %7 offen sc1\n\t"
               "s_mov_b64 exec, %11\n\tbuffer_load_dword %2, %4, %5, %8 offen sc1\n\t"
               "s_mov_b64 exec, -1\n\ts_waitcnt vmcnt(0)"
               : "+v"(d[0]), "+v"(d[1]), "+v"(t)
               : "v"(vd), "v"(vt), "s"(r), "s"(s0), "s"(s1), "s"(st), "s"(m0), "s"(m1), "s"(mt)
               : "memory");
}
DEVI void ldpoll4(v4u* d, const u64* m, rsrc_t r, u32 vd, const u32* sd) {
  asm volatile("s_mov_b64 exec, %10\n\tbuffer_load_dwordx4 %0, %4, %5, %6 offen sc1\n\t"
               "s_mov_b64 exec, %11\n\tbuffer_load_dwordx4 %1, %4, %5, %7 offen sc1\n\t"
               "s_mov_b64 exec, %12\n\tbuffer_load_dwordx4 %2, %4, %5, %8 offen sc1\n\t"
               "s_mov_b64 exec, %13\n\tbuffer_load_dwordx4 %3, %4, %5, %9 offen sc1\n\t"
               "s_mov_b64 exec, -1\n\ts_waitcnt vmcnt(0)"
               : "+v"(d[0]), "+v"(d[1]), "+v"(d[2]), "+v"(d[3])
               : "v"(vd), "s"(r), "s"(sd[0]), "s"(sd[1]), "s"(sd[2]), "s"(sd[3]), "s"(m[0]), "s"(m[1]), "s"(m[2]), "s"(m[3])
               : "memory");
}
DEVI void ldpoll1(v4u& d, u64 m, rsrc_t r, u32 vd, u32 sd) {
  asm volatile("s_mov_b64 exec, %4\n\tbuffer_load_dwordx4 %0, %1, %2, %3 offen sc1\n\ts_mov_b64 exec, -1\n\ts_waitcnt vmcnt(0)"
               : "+v"(d) : "v"(vd), "s"(r), "s"(sd), "s"(m) : "memory");
}
DEVI u32 now() { return (u32)__builtin_amdgcn_s_memrealtime(); }
DEVI void until(u32 t) {
  u32 c;
  asm volatile("s_movk_i32 %0, 0x100\n"
               "1:\n\ts_memrealtime vcc\n\ts_waitcnt lgkmcnt(0)\n\ts_sub_u32 vcc_lo, vcc_lo, %1\n\ts_cmp_ge_i32 vcc_lo, 0\n\ts_cbranch_scc1 2f\n\t"
               "s_sub_u32 %0, %0, 1\n\ts_cbranch_scc0 1b\n"
               "2:" : "=&s"(c) : "s"(t) : "vcc", "scc", "memory");
}
template <int C> DEVI int dmax(int x) { return max(x, __builtin_amdgcn_update_dpp(0, x, C, 0xf, 0xf, true)); }
DEVI u64 bal(bool c) { return __builtin_amdgcn_ballot_w64(c); }
DEVI float mx(float a, float b) { return __builtin_elementwise_maximum(a, b); }
#define SB() __builtin_amdgcn_sched_barrier(0)
#define PIN(x) asm volatile("" : "+v"(x))
#define VMW(n) asm volatile("s_waitcnt vmcnt(%0)" ::"n"(n) : "memory")

#define STAMPA(i)
#define STAMPB(i)
#define STAMP(i)
#define STAMPH(i)
#define STAMPK(i)
#define CNT(x)

DEVI v4u tagged(v4f v, u32 tag) {
  return (v4u){__builtin_amdgcn_perm(fu(v.x), tag, 0x07060500u), __builtin_amdgcn_perm(fu(v.y), tag, 0x07060501u),
               __builtin_amdgcn_perm(fu(v.z), tag, 0x07060502u), __builtin_amdgcn_perm(fu(v.w), tag, 0x07060503u)};
}
DEVI u32 tagof(v4u d) {
  return __builtin_amdgcn_perm(__builtin_amdgcn_perm(d.w, d.z, 0x0c0c0400u), __builtin_amdgcn_perm(d.y, d.x, 0x0c0c0400u), 0x05040100u);
}
DEVI v4f untag(v4u d) { return (v4f){uf(d.x & ~255u), uf(d.y & ~255u), uf(d.z & ~255u), uf(d.w & ~255u)}; }
DEVI u32 sum4(v4u d) { return d.x + d.y + d.z + d.w; }
template <int T> DEVI v4u onehot(u32 v, const u32* msk) {
  return (v4u){dppu<0x150 + T>(v) & msk[0], dppu<0x150 + T>(v) & msk[1], dppu<0x150 + T>(v) & msk[2], dppu<0x150 + T>(v) & msk[3]};
}
template <int I, int N, class F> DEVI void sfor(F&& f) {
  if constexpr (I < N) { f(std::integral_constant<int, I>{}); sfor<I + 1, N>(f); }
}
DEVI void lnstat(v4u xa, u32 xb, float& mb, float& rs) {
  const float pv = uf(__builtin_amdgcn_readfirstlane(xa.x) << 16);
  float xv[10] = {bflo(xa.x), bfhi(xa.x), bflo(xa.y), bfhi(xa.y), bflo(xa.z), bfhi(xa.z), bflo(xa.w), bfhi(xa.w), bflo(xb), bfhi(xb)};
  float s1a = 0.f, s1b = 0.f, s2a = 0.f, s2b = 0.f;
#pragma unroll
  for (int i = 0; i < 5; ++i) { float d = xv[2 * i] - pv, e = xv[2 * i + 1] - pv; s1a += d; s1b += e; s2a += d * d; s2b += e * e; }
  float s = sw32add(s1a + s1b, s2a + s2b);
  s = sw16add(s, s);
  s = rowsum(s);
  const float m1 = uf(__builtin_amdgcn_readlane(fu(s), 0)) * (1.0f / 640.0f), m2 = uf(__builtin_amdgcn_readlane(fu(s), 32)) * (1.0f / 640.0f);
  const float var = fmaxf(m2 - m1 * m1, 0.f);
  mb = rb(pv + m1);
  rs = __builtin_amdgcn_rcpf(rb(__builtin_amdgcn_sqrtf(rb(rb(var) + 1e-5f))));
}

#define PAGE() asm volatile("s_branch 1f\n\t.p2align 12\n1:")
__global__ void __launch_bounds__(512, 2) __attribute__((aligned(4096)))
mla_decode_kernel(const void* __restrict__ px, const void* __restrict__ pckv, const void* __restrict__ pwq, const void* __restrict__ pwuk,
                  void* __restrict__ pws, const void* __restrict__ pwuv, const void* __restrict__ pwo, const void* __restrict__ pw1,
                  const void* __restrict__ pw2, void* __restrict__ pout) {
  __shared__ __attribute__((aligned(1024))) char smem[L_END];
  u64 pc;
  asm volatile("s_getpc_b64 %0" : "=s"(pc));
  asm volatile("" ::"v"((LAS char*)smem));
  STAMP(0);
  const u32 tstart = now();
  const u32 tid = threadIdx.x, lane = tid & 63, n = lane & 15, g = lane >> 4;
  const u32 w = __builtin_amdgcn_readfirstlane(tid >> 6);
  const u32 b = blockIdx.x, xcd = b & 7, r = b >> 3;
  if (CODE_BYTES && w == 7) dma16(mk((const void*)(pc & ~255ull), CODE_BYTES), lane * 16, r * 1024, r < 16 ? L_X + 2048 : L_PFD + 7 * 1024);
  asm volatile("" ::"s"(px), "s"(pckv), "s"(pwq), "s"(pwuk), "s"(pwuv), "s"(pwo), "s"(pw1), "s"(pw2), "s"(pout), "s"(pws));
  const rsrc_t rws = mk(pws, WS_BYTES);
  const rsrc_t rx = mk(px, 1280);
  u64 ep;
  auto ep_load = [&] { asm volatile("s_load_dwordx2 %0, %1, 0x0" : "=s"(ep) : "s"(pws) : "memory"); };
  u32 tlo, thi, tgh;
  auto ep_use = [&] {
    asm volatile("" : "+s"(ep));
    tlo = (u32)(ep + 1); thi = (u32)((ep + 1) >> 32); tgh = tlo * 0x9e3779b1u + thi * 0x85ebca6bu;
  };

  if (__builtin_expect(r < 16, 1)) {
    const u32 h = xcd * 4 + (r & 3), cq = r >> 2, wi = h * 4 + cq;
    const rsrc_t rwq = mk(pwq, 640u * 4096 * 2), rwuk = mk(pwuk, 512u * 4096 * 2);
    if (w == 0) { dma16(rx, lane * 16, 0, L_X); dma16(rx, lane * 16, 1024, L_X + 1024); }
    if (w == 1) __builtin_amdgcn_raw_buffer_store_b32(~0u, rws, lane * 4, WS_QA + cq * 8192 + h * 256, 16);
    SB();
    v4u wq[20], wuk[4];
    const u32 wq_v = (4 * w + g) * 8192 + h * 256 + n * 16, wuk_v = g * 8192 + ((n ^ g) << 4);
    auto fld = [&](auto ic) {
      constexpr int i = ic;
      if constexpr (i >= UP && i < UP + 4) {
        dma16(rwuk, wuk_v ^ ((i - UP) << 6), (cq * 128 + 16 * w + 4 * (i - UP)) * 8192 + h * 256, L_UK + w * 4096 + (i - UP) * 1024);
      } else {
        constexpr int t = i < UP ? i : i - 4;
        u32 tt = t + 5 * cq;
        tt = tt >= 20 ? tt - 20 : tt;
        wq[t] = ld16p(rwq, wq_v, tt * 262144);
      }
    };
    sfor<0, WD>(fld);
    SB();
    STAMP(1);
    ep_load();
    u32 msk[4];
#pragma unroll
    for (int q = 0; q < 4; ++q) { msk[q] = (q == (int)((n & 7) >> 1)) ? ((n & 1) ? 0xffff0000u : 0x0000ffffu) : 0u; PIN(msk[q]); }
    u32 a_qw = L_QP + w * 320 + n * 16 + g * 8, a_qr = L_QP + (n & 7) * 320 + g * 16, a_pub = WS_QA + cq * 8192 + h * 256 + (((2 * w + (n >> 3)) ^ sig(h & 15)) << 4) + (n & 7) * 2;
    u32 t0 = n + 5 * cq, t1 = 16 + (n & 3) + 5 * cq;
    t0 = t0 >= 20 ? t0 - 20 : t0; t1 = t1 >= 20 ? t1 - 20 : t1;
    u32 a_x = L_X + lane * 16, a_xb = L_X + 1024 + lane * 4, a_x0 = L_X + (32 * t0 + 4 * w + g) * 2, a_x1 = L_X + (32 * t1 + 4 * w + g) * 2;
    u32 a_uk = L_UK + w * 4096 + n * 256 + ((g ^ n) << 4);
    PIN(a_qw); PIN(a_qr); PIN(a_pub); PIN(a_x); PIN(a_xb); PIN(a_x0); PIN(a_x1); PIN(a_uk);
    SB();
    VMW(WD);
    bar();
    ep_use();
    if (w == 1 && lane == 0) st8((v2u){tlo, thi}, rws, 0, WS_FL + wi * 8);
    u32 x0, x1;
    {
      const v4u xa = lds_ld<v4u>(a_x);
      const u32 xb = lds_ld<u32>(a_xb), xg0 = lds_ld<unsigned short>(a_x0), xg1 = lds_ld<unsigned short>(a_x1);
      SB();
      float mb, rs;
      lnstat(xa, xb, mb, rs);
      const u32 dp = pk(bflo(xg0) - mb, bflo(xg1) - mb);
      const float a0 = bflo(dp) * rs, a1 = bfhi(dp) * rs;
      x0 = pk(a0, a0); x1 = pk(a1, a1);
    }
    PIN(x0); PIN(x1);
    SB();
    STAMP(4);
    auto touch = [&](int q) {
      const u32 kp = HHS ? r & 7 : r >> 1, hh = HHS ? r >> 3 : r & 1;
      asm volatile("s_mov_b32 m0, %3\n\ts_nop 0\n\tbuffer_load_dword %0, %1, %2 offen lds" ::"v"((lane >> 2) * 1024 + (lane & 3) * 64), "s"(mk(pckv, 16384u * 1024)),
                   "s"(((kp * 8 + xcd) * 256 + hh * 128 + w * 16) * 1024 + q * 256), "s"(L_QP + 4096 + w * 256) : "memory");
    };
    static_assert(WPE <= WD, "");
    v4f qa = {0.f, 0.f, 0.f, 0.f};
    sfor<0, 24>([&](auto ic) {
      constexpr int i = ic, t = i < UP ? i : i - 4;
      constexpr bool isq = i < UP || i >= UP + 4;
      v4u oh;
      if constexpr (isq) { oh = onehot<(t & 15)>(t < 16 ? x0 : x1, msk); PIN(oh); }
      VMW(i + WD <= 24 ? WD - 1 : 23 - i + (i > 24 - WPE));
      if constexpr (i + WD < 24) fld(std::integral_constant<int, i + WD>{});
      if constexpr (i == 24 - WPE) touch(4 - WPF);
      if constexpr (isq) { PIN(wq[t]); qa = mfma(oh, wq[t], qa); }
      if constexpr (i == UP + 3) {
#pragma unroll
        for (int u = 0; u < 4; ++u) wuk[u] = lds_ld<v4u>(a_uk ^ (u << 6));
      }
      if constexpr (i == 0) STAMP(3);
      if constexpr (i + WD == 23) STAMP(5);
      SB();
    });
    if (g < 2) lds_st<v2u>(a_qw, (v2u){pk(qa.x, qa.y), pk(qa.z, qa.w)});
    STAMP(6);
    bar();
    {
      v4u qf[4];
#pragma unroll
      for (int u = 0; u < 4; ++u) qf[u] = lds_ld<v4u>(a_qr + u * 64);
      v4f qb = {0.f, 0.f, 0.f, 0.f};
#pragma unroll
      for (int u = 0; u < 4; ++u) qb = mfma(qf[u], wuk[u], qb);
      float v = (qb.x + qb.y) + (qb.z + qb.w);
      v = sw16add(v, v) * (0.08838834764831845f * 1.4426950408889634f);
      const u32 p = pk(v, dpp<0xb1>(v));
      if ((lane & 0x31) == 0) __builtin_amdgcn_raw_buffer_store_b32(min(p, ~1u), rws, a_pub, 0, 16);
    }
    SB();
    STAMP(7);
#pragma unroll
    for (int q = 4 - WPF + (WPE != 0); q < 4; ++q) touch(q);
    SB();
    const rsrc_t rwuv = mk(pwuv, 512u * 4096 * 2), rwo = mk(pwo, 4096u * 640 * 2);
    const u32 uv_v = g * 8192 + ((n ^ ((((w & 3) << 1) | (g & 1)) << 1)) << 4), wo_v = g * 1280 + ((n ^ (((w & 1) * 4 + g) << 1)) << 4);
    const u32 q4 = n >> 2, p4 = n & 3;
    u32 a_wo[5], a_uv = L_UV + (16 * q4 + g) * 256 + ((w ^ ((q4 << 1) | (g & 1))) << 5) + p4 * 8;
#pragma unroll
    for (int k = 0; k < 5; ++k) {
      const u32 rho = 5 * w + (k < 4 ? p4 : 4), i = k < 4 ? k : p4;
      const u32 c4 = (rho >> 1) * 8 + 2 * i + (rho & 1);
      a_wo[k] = L_WO + (g * 5 + (c4 >> 5)) * 1024 + q4 * 256 + ((((c4 >> 2) & 7) ^ ((g & 1) * 4 + q4)) << 5) + (c4 & 3) * 8;
      PIN(a_wo[k]);
    }
    u32 a_cw = L_CP + w * 320 + n * 16 + g * 8, a_cr = L_CP + (n & 7) * 320 + g * 16, a_mr = L_ML + (g & 1) * 16;
    u32 a_xw = L_CX + ((((w >> 1) - cq) & 3) * 32 + (n >> 2) * 8 + (w & 1) * 4 + (n & 3)) * 2, a_xr = L_CX + g * 16;
    u32 a_in = g * OSK + n * 16, a_so = (lane >> 2) * 16384 + (lane & 3) * 16, a_ml = (4 * (n & 1) + g) * 512;
    u32 a_cs = (4 * (n & 1) + g) * 4096 + (n >> 3) * 256 + (((h >> 2) & 3) * 16 + (h & 1) * 8 + (n & 6) + ((h >> 1) & 1)) * 4;
    PIN(a_uv); PIN(a_cw); PIN(a_cr); PIN(a_mr); PIN(a_xw); PIN(a_xr); PIN(a_in); PIN(a_so); PIN(a_ml); PIN(a_cs);
    v4u uv[4], wo[4][5], ml, ts;
    asm volatile("" : "=v"(ml));
    const u32 s_ml = WS_ML + w * 4096 + h * 16, s_ts = WS_TS + (h >> 4) * 1024 + w * 128;
    u32 a_t8 = (lane & 7) * 16, a_lm = L_ML + w * 4 + min(lane, 2u) * 32;
    PIN(a_t8); PIN(a_lm);
    SB();
    if (PG & 1) PAGE();
    until(tstart + BKT);
    static_assert(9 + BD <= 19 && UVP % 5 == 0 && UVP >= 5 && UVP <= 15, "quarter 3 reuses the slot of quarter 0");
    sfor<0, 24 + BD>([&](auto ic) {
      constexpr int i = ic;
      if constexpr (i >= BD) VMW(i < 24 ? BD - 1 : 23 - (i - BD) + (MLE && i > 23 + MLE));
      if constexpr (i >= 24) {
        if constexpr (i == 23 + MLE) ts = ld16a(rws, a_t8, s_ts);
      } else if constexpr (i >= UVP && i < UVP + 4) {
        dma16(rwuv, uv_v, (128 * (w & 3) + 32 * cq + 16 * (w >> 2) + 4 * (i - UVP)) * 8192 + h * 256, L_UV + (4 * w + i - UVP) * 1024);
      } else {
        constexpr int j = i < UVP ? i : i - 4, u = j / 5, mb = j % 5;
        dma16(rwo, wo_v, (h * 128 + ((u + cq) & 3) * 32 + 4 * w) * 1280 + mb * 256, L_WO + (((u % 3) * 8 + w) * 5 + mb) * 1024);
      }
      constexpr int d = i - BD, dj = d < UVP ? d : d - 4;
      if constexpr (i == 0) STAMPA(14);
      if constexpr (i == BD - 1) STAMPA(2);
      if constexpr (d == UVP + 3) {
        STAMPA(13);
        bar();
#pragma unroll
        for (int u = 0; u < 4; ++u) uv[u] = trd2(a_uv + (64 * (u >> 1) + 4 * (u & 1)) * 256, a_uv + (64 * (u >> 1) + 4 * (u & 1) + 8) * 256);
        SB();
      } else if constexpr (d >= 0 && !(d >= UVP && d < UVP + 4) && dj % 5 == 4) {
        constexpr int u = dj / 5;
        if constexpr (u < 3) STAMPA(10 + u);
        bar();
        if constexpr (u == 3) STAMP(8);
#pragma unroll
        for (int k = 0; k < 5; ++k) wo[u][k] = trd2(a_wo[k] + (u % 3) * 40960, a_wo[k] + (u % 3) * 40960 + 20480);
        SB();
      }
    });
#pragma unroll
    for (int u = 0; u < 4; ++u) {
      PIN(uv[u]);
#pragma unroll
      for (int k = 0; k < 5; ++k) PIN(wo[u][k]);
    }
    SB();
    STAMP(9);
    const u32 s_cs = WS_CS + w * 32768 + (h >> 4) * 2048 + cq * 512, s_in = WS_IN3 + h * OSH + cq * OSC + 8 * w * OSK;
    v4u pv[2], af[2];
    {
      u32 sp = 0;
      VMW(0); PIN(ts);
      u64 bad = bal((ts.z != tlo) | (ts.w != thi));
      _Pragma("nounroll") while (__builtin_expect(bad != 0, 0) && ++sp < SPIN_CAP) {
        __builtin_amdgcn_s_sleep(TS_SLEEP);
        ldpoll1(ts, bad, rws, a_t8, s_ts);
        bad &= bal((ts.z != tlo) | (ts.w != thi));
        CNT(nsp2);
      }
    }
    const u32 tk = tstart + __builtin_amdgcn_readfirstlane(dmax<0x141>(dmax<0x4e>(dmax<0xb1>((int)(ts.x - tstart)))));
    STAMPB(14);
    SB();
    until(tk + DML);
    asm volatile("s_mov_b64 exec, %4\n\tbuffer_load_dwordx4 %0, %1, %2, %3 offen sc1\n\ts_mov_b64 exec, -1"
                 : "+v"(ml) : "v"(a_ml), "s"(rws), "s"(s_ml), "s"(0x0003000300030003ull) : "memory");
    until(tk + DO);
#pragma unroll
    for (int i = 0; i < 2; ++i) pv[i] = ld16a(rws, a_in, s_in + 4 * i * OSK);
    u32 cv = ld4a(rws, a_cs, s_cs);
    SB();
    STAMPB(2);
    VMW(3);
    PIN(ml);
    u64 badm = bal((ml.z != tlo) | (ml.w != thi)) & 0x0003000300030003ull;
    auto mlc = [&] {
      float mw = mx(uf(ml.x), dpp<0xb1>(uf(ml.x)));
      { auto q = __builtin_amdgcn_permlane16_swap(fu(mw), fu(mw), false, false); mw = mx(uf(q[0]), uf(q[1])); }
      { auto q = __builtin_amdgcn_permlane32_swap(fu(mw), fu(mw), false, false); mw = mx(uf(q[0]), uf(q[1])); }
      const float e = __builtin_amdgcn_exp2f(uf(ml.x) - mw);
      const u32 ee = pk(e, e);
      float lw = bfhi(ee) * uf(ml.y);
      lw += dpp<0xb1>(lw);
      lw = sw16add(lw, lw); lw = sw32add(lw, lw);
      lds_st<float>(a_lm, lane ? lw : mw);
      af[0] = onehot<0>(ee, msk); af[1] = onehot<1>(ee, msk);
      PIN(af[0]); PIN(af[1]);
    };
    mlc();
    SB();
    v4f c;
    u64 bad;
    auto ochk = [&](auto first) {
      u32 t[2];
      c = (v4f){0.f, 0.f, 0.f, 0.f};
      sfor<0, 2>([&](auto ic) {
        constexpr int i = ic;
        if constexpr (first) VMW(2 - i);
        PIN(pv[i]);
        c = mfma(af[i], pv[i], c);
        t[i] = sum4(pv[i]);
        t[i] += dppu<0xb1>(t[i]);
        if constexpr (first) SB();
      });
      if constexpr (first) VMW(0);
      PIN(cv);
      bad = bal(((n & 1) ? t[1] : t[0]) + tgh != cv);
    };
    ochk(std::true_type{});
    if (__builtin_expect((bad | badm) != 0, 0)) {
      u32 sp = 0;
      _Pragma("nounroll") while (badm && ++sp < SPIN_CAP) {
        if (ML_SLEEP) __builtin_amdgcn_s_sleep(ML_SLEEP);
        ldpoll1(ml, badm, rws, a_ml, s_ml);
        badm &= bal((ml.z != tlo) | (ml.w != thi));
      }
      mlc();
      bad |= 1;
      _Pragma("nounroll") do {
        ldpoll(pv, cv, (bad & 0x5555555555555555ull) * 3, ((bad >> 1) & 0x5555555555555555ull) * 3, bad, rws, a_in, s_in, s_in + 4 * OSK, a_cs, s_cs);
        ochk(std::false_type{});
        CNT(nsp3);
      } while (bad && ++sp < SPIN_CAP);
    }
    STAMPB(10);
    if (g < 2) lds_st<v2u>(a_cw, (v2u){pk(c.x, c.y), pk(c.z, c.w)});
    STAMPB(11);
    bar();
    {
      v4u cf[4];
#pragma unroll
      for (int u = 0; u < 4; ++u) cf[u] = lds_ld<v4u>(a_cr + u * 64);
      const v4f m4 = lds_ld<v4f>(a_mr), l4 = lds_ld<v4f>(a_mr + 32);
      v4f cx = {0.f, 0.f, 0.f, 0.f};
#pragma unroll
      for (int u = 0; u < 4; ++u) cx = mfma(cf[u], uv[u], cx);
      const float mg = sw16max(fmaxf(fmaxf(m4.x, m4.y), fmaxf(m4.z, m4.w)));
      const float e0 = __builtin_amdgcn_exp2f(m4.x - mg), e1 = __builtin_amdgcn_exp2f(m4.y - mg);
      const float e2 = __builtin_amdgcn_exp2f(m4.z - mg), e3 = __builtin_amdgcn_exp2f(m4.w - mg);
      float den = (e0 * l4.x + e1 * l4.y) + (e2 * l4.z + e3 * l4.w);
      const float inv = __builtin_amdgcn_rcpf(sw16add(den, den));
      float v = (e0 * cx.x + e1 * cx.y) + (e2 * cx.z + e3 * cx.w);
      v = sw16add(v, v) * inv;
      if (g == 0) lds_st<unsigned short>(a_xw, (unsigned short)pk(v, v));
    }
    STAMPB(12);
    bar();
    {
      v4u xf[4];
#pragma unroll
      for (int u = 0; u < 4; ++u) xf[u] = lds_ld<v4u>(a_xr + u * 64);
      v4f s[5];
#pragma unroll
      for (int k = 0; k < 5; ++k) s[k] = (v4f){0.f, 0.f, 0.f, 0.f};
#pragma unroll
      for (int u = 0; u < 4; ++u)
#pragma unroll
        for (int k = 0; k < 5; ++k) s[k] = mfma(xf[u], wo[u][k], s[k]);
      const u32 s4 = fu(s[4].x);
      const v4f d = {uf(__builtin_amdgcn_update_dpp(fu(s[0].x), s4, 0xe4, 0x2, 0xf, false)), uf(__builtin_amdgcn_update_dpp(fu(s[1].x), s4, 0x104, 0x2, 0xf, false)),
                     uf(__builtin_amdgcn_update_dpp(fu(s[2].x), s4, 0x108, 0x2, 0xf, false)), uf(__builtin_amdgcn_update_dpp(fu(s[3].x), s4, 0x10c, 0x2, 0xf, false))};
      if (lane < 20) st16(tagged(d, tlo), rws, a_so, WS_IN5 + (5 * w * 256 + wi) * 64);
    }
    STAMPB(13);
    if (PG & 4) asm volatile("s_endpgm\n\t.p2align 12");
  } else {
    const u32 hh = HHS ? (r >> 3) & 1 : r & 1, kp = HHS ? r & 7 : (r >> 1) & 7, ks = xcd * 8 + kp, kc = kp * 8 + xcd;
    const u32 fb = xcd + 8 * ((r >> 2) & 3), yi = fb * 4 + (r & 3);
    const rsrc_t rck = mk(pckv, 16384u * 1024);
    constexpr auto hq = [](int j) { return ORD == 2 ? j & 3 : ORD == 4 ? "\0\1\0\2\1\2\3\3"[j] : ORD == 5 ? "\0\1\2\0\1\2\3\3"[j] : ORD == 6 ? "\0\1\0\1\2\2\3\3"[j] : ORD == 3 ? "\0\1\2\0\3\1\2\3"[j] : ORD ? "\0\1\0\2\1\3\2\3"[j] : j >> 1; };
    constexpr auto ht = [](int j) { return ORD == 2 ? j >> 2 : ORD == 4 ? "\0\0\1\0\1\1\0\1"[j] : ORD == 5 ? "\0\0\0\1\1\1\0\1"[j] : ORD == 6 ? "\0\0\1\1\0\1\0\1"[j] : ORD == 3 ? "\0\0\0\1\0\1\1\1"[j] : ORD ? "\0\0\1\0\1\0\1\1"[j] : j & 1; };
    if (w >= 4) {
      dma16(rx, lane * 16, 0, L_P + (w - 4) * 2048); dma16(rx, lane * 16, 1024, L_P + (w - 4) * 2048 + 1024);
      const rsrc_t rwuk = mk(pwuk, 512u * 4096 * 2);
#pragma unroll
      for (int i = 0; i < PF; ++i) dma16(rwuk, lane * 16, ((r - 16) * 32 + 8 * (w - 4) + i) * 8192 + xcd * 1024, L_PFD + w * 1024);
      if (w == 5) lds_st<float>(L_MX + lane * 4, -__builtin_inff());
      if (w == 6) lds_st<v4u>(L_G + lane * 16, (v4u){0u, 0u, 0u, 0u});
    }
    SB();
    ep_load();
    u32 ck_v[4], ra[2][4], rq[4], va[2][2], a_pr[4];
    {
      const u32 q = n >> 2, p = n & 3;
#pragma unroll
      for (int u = 0; u < 4; ++u) {
        ck_v[u] = g * 1024 + ((n ^ (g << 2) ^ u) << 4);
        rq[u] = L_QA + n * 256 + (((g * 4 + u) ^ sig(n)) << 4); ra[0][u] = rq[u] + (L_CKV + w * 4096 - L_QA); ra[1][u] = ra[0][u] + 65536;
        a_pr[u] = L_P + n * 256 + (((4 * u + g) ^ n) << 4);
        PIN(ck_v[u]); PIN(ra[0][u]); PIN(ra[1][u]); PIN(rq[u]); PIN(a_pr[u]);
      }
#pragma unroll
      for (int t = 0; t < 2; ++t) {
        const u32 row = 8 * g + 4 * t + q;
        va[0][t] = L_CKV + row * 256 + (((2 * w + (p >> 1)) ^ sig(row & 15)) << 4) + 8 * (p & 1); va[1][t] = va[0][t] + 65536;
        PIN(va[0][t]); PIN(va[1][t]);
      }
    }
    u32 a_o = (4 * g + (n >> 3)) * OSH + (n & 7) * 16, a_mx = L_MX + n * 4, a_pw = L_P + n * 256 + (((2 * w + (g >> 1)) ^ n) << 4) + (g & 1) * 8;
    u32 a_f1 = (lane >> 1) * 3456 + (lane & 1) * 16, a_q = lane * 16, a_tl = L_L + n * 48, a_ts = WS_CS + ks * 4096 + hh * 2048 + w * 256 + lane * 4;
    u32 a_ms = WS_ML + ks * 512 + hh * 256 + n * 16, a_lw = a_tl + w * 4;
    u32 a_f0 = w < 5 ? a_f1 : a_q;
    PIN(a_o); PIN(a_mx); PIN(a_pw); PIN(a_f0); PIN(a_q); PIN(a_tl); PIN(a_ts); PIN(a_ms); PIN(a_lw);
    const u32 s_o = WS_IN3 + (w >> 1) * OSC + ks * OSK + (w & 1) * 128 + 16 * hh * OSH;
    const u32 ck_s = (kc * 256 + 32 * w) * 1024;
    auto ckld = [&](auto ic) {
      constexpr int j = ic / 8, i = ic % 8;
      dma16s<ht(j)>(rck, ck_v[i & 3], ck_s + ((hh ^ ht(j)) * 128 + 4 * i) * 1024 + hq(j) * 256, L_CKV + (j & 3) * 32768 + (32 * w + 4 * i) * 256);
    };
    constexpr auto isd = [](int d) {
      const int nb = (d + 1) / 8, cap = (nb + 3 > 8 ? 8 : nb < 1 ? 4 : nb + 3) * 8;
      return nb > QM || d + 1 + KD > cap ? cap : d + 1 + KD;
    };
    float xs[5];
    SB();
    if (w < 4) {
      if (KS) __builtin_amdgcn_s_sleep(KS);
      sfor<0, isd(-1)>(ckld);
    } else {
      VMW(PF);
      const v4u xa = lds_ld<v4u>(L_P + (w - 4) * 2048 + lane * 16);
      const u32 xb = lds_ld<u32>(L_P + (w - 4) * 2048 + 1024 + lane * 4);
      float mb, rs;
      lnstat(xa, xb, mb, rs);
#pragma unroll
      for (int i = 0; i < 5; ++i) {
        xs[i] = rb(rb(bflo(lds_ld<unsigned short>(L_P + (w - 4) * 2368 + i * 64 + (lane & 62))) - mb) * rs);
        PIN(xs[i]);
      }
    }
    SB();
    STAMP(1);
    v4u kf[8][4], vt[8][4], qf[16];
    v4f sc[2] = {{0.f, 0.f, 0.f, 0.f}, {0.f, 0.f, 0.f, 0.f}};
    auto kfrd = [&](int j) {
#pragma unroll
      for (int u = 0; u < 4; ++u) kf[j][u] = lds_ld<v4u>(ra[(j >> 1) & 1][u] + (j & 1) * 32768);
    };
    auto vtrd = [&](int j, int kk) { vt[j][kk] = trd2(va[(j >> 1) & 1][0] + (j & 1) * 32768 + kk * 8192, va[(j >> 1) & 1][1] + (j & 1) * 32768 + kk * 8192); };
    rsrc_t rw2 = mk(pw2, fb < 27 ? 640u * 1728 * 2 : 0), rw12 = mk(w < 5 ? pw1 : pw2, fb < 27 ? 640u * 1728 * 2 : 0);
    u32 s_f0 = w < 5 ? (160 * (r & 3) + 32 * w) * 3456 + yi * 32 : yi * 20480 + (11 + w) * 1024, s_f1 = yi * 20480 + w * 1024;
    u32 l_f0 = w < 5 ? L_FF + 20480 + (r & 3) * 5120 + w * 1024 : L_FF + (11 + w) * 1024, l_f1 = L_FF + w * 1024;
    asm volatile("" : "+s"(rw2), "+s"(rw12), "+s"(s_f0), "+s"(s_f1), "+s"(l_f0), "+s"(l_f1));
    SB();
    auto ffn_ld = [&](int k) {
      if (k == 0) dma16(rw12, a_f0, s_f0, l_f0);
      else if (k < 3) dma16(rw2, a_q, s_f1 + (k - 1) * 8192, l_f1 + (k - 1) * 8192);
      else if (w == 7) dma16(rw2, a_q, s_f1 + 12 * 1024, L_FF + 19 * 1024);
      SB();
    };
    static_assert(QM == 5 || FFA, "");
    static_assert(QM > QB && QM >= 3 && QM <= 7 && FB < QB, "");
    sfor<0, 8>([&](auto jc) {
      constexpr int j = jc;
      if (w < 4) {
        sfor<8 * j, 8 * j + 7>([&](auto dc) {
          constexpr int d = dc;
          if constexpr (isd(d) > isd(d - 1)) { VMW(isd(d - 1) - d - 1); sfor<isd(d - 1), isd(d)>(ckld); }
        });
        VMW(isd(8 * j + 6) - 8 * j - 8);
      }
      if constexpr (j == PGJ && (PG & 2)) PAGE();
      if constexpr (j == 7) STAMPK(3);
      if constexpr (j <= QM || j <= 4) bar();
      else if constexpr (j == 7 && FFA == 0) asm volatile("s_waitcnt lgkmcnt(12)\n\ts_barrier" ::: "memory");
      else __builtin_amdgcn_s_barrier();
      STAMPH(2 + j);
      if constexpr (isd(8 * j + 7) > isd(8 * j + 6)) { if (w < 4) sfor<isd(8 * j + 6), isd(8 * j + 7)>(ckld); }
      if constexpr (j == FB) {
        if (w >= 4) asm volatile("s_mov_b32 m0, %3\n\ts_nop 0\n\tbuffer_load_dword %0, %1, %2 offen sc1 lds" ::"v"((lane & 31) * 4), "s"(rws),
                                 "s"(WS_FL + hh * 512 + (w - 4) * 128), "s"(L_P + w * 256) : "memory");
      }
      if constexpr (j == TSB) {
        if (w == 4 && lane == 0) st16((v4u){now(), 0u, tlo, thi}, rws, 0, WS_TS + hh * 1024 + ks * 16);
      }
      if constexpr (j == QM) STAMPK(5);
      SB();
      if constexpr (j == QM) sfor<0, SD>([&](auto uc) { constexpr int u = uc; qf[u] = lds_ld<v4u>(rq[u & 3] + (u >> 2) * 4096); SB(); });
      kfrd(j);
      SB();
      if constexpr (j == 7 && FFA == 0) ffn_ld(0);
      if constexpr (j == 7 && FFA == 0 && FFC >= 1) ffn_ld(1);
      if constexpr (j == 7 && FFA == 0 && FFC >= 2) ffn_ld(2);
      if constexpr ((j < 4 && j != QM) || (j > QM && (j < 7 || !VTL))) {
#pragma unroll
        for (int kk = 0; kk < 4; ++kk) vtrd(j, kk);
      }
      if constexpr (j == 0) ep_use();
      SB();
      if constexpr (j == QB) {
        if (w >= 4) {
          VMW(0);
          if (QS) __builtin_amdgcn_s_sleep(QS);
#pragma unroll
          for (int i = 0; i < 4; ++i)
            asm volatile("s_mov_b32 m0, %3\n\ts_nop 0\n\tbuffer_load_dwordx4 %0, %1, %2 offen sc1 lds" ::"v"(a_q), "s"(rws),
                         "s"(WS_QA + hh * 4096 + (w - 4) * 1024 + i * 8192), "s"(L_QA + (w - 4) * 1024 + i * 4096) : "memory");
          STAMPK(2);
        }
        SB();
      }
      if constexpr (j == QM - 1) {
        if (w >= 4) {
          const u32 a_qs = a_q + L_QA + (w - 4) * 1024;
          v2u fv = lds_ld<v2u>(L_P + w * 256 + n * 8);
          u64 bf = bal((fv.x != tlo) | (fv.y != thi)), bq[4];
          v4u qr[4];
          SB();
          sfor<0, 4>([&](auto ic) { VMW(3 - ic); qr[ic] = lds_ld<v4u>(a_qs + ic * 4096); SB(); });
          u32 qm[4];
#pragma unroll
          for (int i = 0; i < 4; ++i) qm[i] = max(max(qr[i].x, qr[i].y), max(qr[i].z, qr[i].w));
          STAMPK(4);
          if (__builtin_expect((bf | bal(max(max(qm[0], qm[1]), max(qm[2], qm[3])) == ~0u)) != 0, 0)) {
            const u32 sq = WS_QA + hh * 4096 + (w - 4) * 1024, s_q[4] = {sq, sq + 8192, sq + 16384, sq + 24576};
            u32 sp = 0;
#pragma unroll
            for (int i = 0; i < 4; ++i) bq[i] = bal(qm[i] == ~0u);
            if (bf) {
              _Pragma("nounroll") do {
                asm volatile("" ::: "memory");
                fv = __builtin_amdgcn_raw_buffer_load_b64(rws, n * 8, WS_FL + hh * 512 + (w - 4) * 128, 16);
              } while (bal((fv.x != tlo) | (fv.y != thi)) && ++sp < SPIN_CAP);
              bq[0] = bq[1] = bq[2] = bq[3] = ~0ull;
            }
            _Pragma("nounroll") do {
              if (QRS) __builtin_amdgcn_s_sleep(QRS);
              ldpoll4(qr, bq, rws, a_q, s_q);
#pragma unroll
              for (int i = 0; i < 4; ++i) {
                lds_st<v4u>(a_qs + i * 4096, qr[i]);
                bq[i] = bal(max(max(qr[i].x, qr[i].y), max(qr[i].z, qr[i].w)) == ~0u);
              }
              CNT(nsp2);
            } while ((bq[0] | bq[1] | bq[2] | bq[3]) && ++sp < SPIN_CAP);
          }
        }
        SB();
      }
      if constexpr (j == QM) {
        sfor<0, 16>([&](auto uc) {
          constexpr int u = uc;
          sfor<0, QM + 1>([&](auto ic) { constexpr int i = ic; if constexpr (hq(i) == u >> 2) sc[ht(i)] = mfma(kf[i][u & 3], qf[u], sc[ht(i)]); });
          if constexpr (u + SD < 16) qf[u + SD] = lds_ld<v4u>(rq[u & 3] + ((u + SD) >> 2) * 4096);
          constexpr int j0 = QM < 4 ? QM : 4, nv = 4 * (QM + 1 - j0), v0 = (u + SD - 16) * nv / SD, v1 = (u + 1 + SD - 16) * nv / SD;
          if constexpr (u + SD >= 16) sfor<v0, v1>([&](auto vc) { vtrd(j0 + vc / 4, vc % 4); });
          SB();
        });
        STAMPA(10);
      }
      sfor<j - 1, j + (j == 7)>([&](auto ic) {
        constexpr int i = ic;
        if constexpr (i > QM) {
#pragma unroll
          for (int u = 0; u < 4; ++u) sc[ht(i)] = mfma(kf[i][u], qf[4 * hq(i) + u], sc[ht(i)]);
          SB();
        }
      });
    });
    static_assert(SD % 4 == 0, "");
    if (FFA == 0 && FFC == 0) ffn_ld(1);
    if (FFA == 0 && FFC == 1) ffn_ld(2);
    STAMPK(6);
    __hip_atomic_fetch_max((LAS float*)a_mx, mx(mx(mx(sc[0].x, sc[0].y), mx(sc[0].z, sc[0].w)), mx(mx(sc[1].x, sc[1].y), mx(sc[1].z, sc[1].w))),
                           __ATOMIC_RELAXED, __HIP_MEMORY_SCOPE_WORKGROUP);
    STAMPK(7);
    bar();
    const float mrow = lds_ld<float>(a_mx);
    SB();
    if constexpr (VTL && QM < 7) {
#pragma unroll
      for (int kk = 0; kk < 4; ++kk) vtrd(7, kk);
      SB();
    }
    if (FFA == 1) { ffn_ld(0); ffn_ld(1); }
    if (FFA == 1 || FFC == 0) ffn_ld(2);
    ffn_ld(3);
    {
      float ls = 0.f;
#pragma unroll
      for (int t = 0; t < 2; ++t) {
        const float p0 = __builtin_amdgcn_exp2f(sc[t].x - mrow), p1 = __builtin_amdgcn_exp2f(sc[t].y - mrow);
        const float p2 = __builtin_amdgcn_exp2f(sc[t].z - mrow), p3 = __builtin_amdgcn_exp2f(sc[t].w - mrow);
        lds_st<v2u>(a_pw + t * 4096, (v2u){pk(p0, p1), pk(p2, p3)});
        ls += (p0 + p1) + (p2 + p3);
      }
      ls = sw32add(ls, ls);
      lds_st<float>(a_lw, sw16add(ls, ls));
    }
    STAMPK(8);
    if (PVS && w >= 4) __builtin_amdgcn_s_setprio(1);
    bar();
    v4u pf[2][4];
#pragma unroll
    for (int t = 0; t < 2; ++t)
#pragma unroll
      for (int kk = 0; kk < 4; ++kk) pf[t][kk] = lds_ld<v4u>(a_pr[kk] + t * 4096);
    auto mlst = [&] {
      if (w == MLW) {
        const v4f l4 = lds_ld<v4f>(a_tl) + lds_ld<v4f>(a_tl + 16);
        if (g == 0) st16((v4u){fu(mrow), fu((l4.x + l4.y) + (l4.z + l4.w)), tlo, thi}, rws, a_ms, 0);
      }
      SB();
    };
    SB();
    if (MLP == 0) mlst();
    STAMPA(11);
    {
      v4f o[4];
#pragma unroll
      for (int ct = 0; ct < 4; ++ct) o[ct] = (v4f){0.f, 0.f, 0.f, 0.f};
      sfor<0, 2>([&](auto tc) {
#pragma unroll
        for (int kk = 0; kk < 4; ++kk)
          sfor<0, 8>([&](auto jc) { constexpr int j = jc; if constexpr (ht(j) == tc) o[hq(j)] = mfma(pf[tc][kk], vt[j][kk], o[hq(j)]); });
        SB();
        if (MLP == 1 + tc) mlst();
      });
      STAMPA(12);
      u32 cs;
#pragma unroll
      for (int pr = 0; pr < 2; ++pr) {
        const u32 x0 = pk(o[0][2 * pr], o[1][2 * pr]), x1 = pk(o[2][2 * pr], o[3][2 * pr]);
        const u32 y0 = pk(o[0][2 * pr + 1], o[1][2 * pr + 1]), y1 = pk(o[2][2 * pr + 1], o[3][2 * pr + 1]);
        const v4u d = {(u32)__builtin_amdgcn_update_dpp(x0, y0, 0x128, 0xf, 0xc, false), (u32)__builtin_amdgcn_update_dpp(x1, y1, 0x128, 0xf, 0xc, false),
                       (u32)__builtin_amdgcn_update_dpp(y0, x0, 0x128, 0xf, 0x3, false), (u32)__builtin_amdgcn_update_dpp(y1, x1, 0x128, 0xf, 0x3, false)};
        st16(d, rws, a_o, s_o + 2 * pr * OSH);
        u32 t = sum4(d);
        t += dppu<0xb1>(t);
        cs = pr && !(n & 1) ? cs : t;
      }
      __builtin_amdgcn_raw_buffer_store_b32(cs + tgh, rws, a_ts, 0, 16);
      SB();
      if (MLP == 3) mlst();
    }
    STAMPK(9);
    if (fb < 27) {
      VMW(3);
      __builtin_amdgcn_s_barrier();
      if (w >= 4) {
        if (w - 4 != (r & 3)) {
          const rsrc_t rw1 = mk(pw1, 640u * 1728 * 2);
#pragma unroll
          for (int i = 0; i < 5; ++i) dma16(rw1, a_f1, (160 * (w - 4) + 32 * i) * 3456 + yi * 32, L_FF + w * 5120 + i * 1024);
          VMW(0);
        }
        v4u w1r[5];
#pragma unroll
        for (int i = 0; i < 5; ++i) w1r[i] = lds_ld<v4u>(a_q + L_FF + w * 5120 + i * 1024);
        float ha[8];
#pragma unroll
        for (int e = 0; e < 8; ++e) ha[e] = 0.f;
#pragma unroll
        for (int i = 0; i < 5; ++i) {
#pragma unroll
          for (int q = 0; q < 4; ++q) { ha[2 * q] += xs[i] * bflo(w1r[i][q]); ha[2 * q + 1] += xs[i] * bfhi(w1r[i][q]); }
        }
        float h0 = sw16add(sw32add(ha[0], ha[2]), sw32add(ha[1], ha[3]));
        float h1 = sw16add(sw32add(ha[4], ha[6]), sw32add(ha[5], ha[7]));
        h0 += dpp<0x128>(h0); h1 += dpp<0x128>(h1); h0 += dpp<0x124>(h0); h1 += dpp<0x124>(h1); h0 += dpp<0x122>(h0); h1 += dpp<0x122>(h1);
        if (n < 4) lds_st<float>(L_QA + (w - 4) * 64 + ((n & 1) * 8 + (n >> 1) * 4 + g) * 4, n < 2 ? h0 : h1);
      }
      bar();
      if (w < 5) {
        v4u w2r[4];
#pragma unroll
        for (int i = 0; i < 4; ++i) w2r[i] = lds_ld<v4u>(L_FF + (4 * i + g) * 1280 + w * 256 + n * 16);
        float hs = 0.f;
#pragma unroll
        for (int v = 0; v < 4; ++v) hs += lds_ld<float>(L_QA + v * 64 + (4 * (n & 3) + g) * 4);
        hs = fmaxf(hs, 0.f);
        const u32 hv = pk(hs, hs);
        u32 msk[4];
#pragma unroll
        for (int q = 0; q < 4; ++q) msk[q] = (q == (int)((n & 7) >> 1)) ? ((n & 1) ? 0xffff0000u : 0x0000ffffu) : 0u;
        v4f y = mfma(onehot<0>(hv, msk), w2r[0], (v4f){0.f, 0.f, 0.f, 0.f});
        y = mfma(onehot<1>(hv, msk), w2r[1], y);
        y = mfma(onehot<2>(hv, msk), w2r[2], y);
        y = mfma(onehot<3>(hv, msk), w2r[3], y);
        if (g < 2) st16(tagged(y, tlo), rws, ((n >> 2) * 2 + g) * 16384 + (n & 3) * 16, WS_IN5 + (w * 8 * 256 + 128 + yi) * 64);
        STAMPB(12);
      }
    } else if (w < 4) {
      if (PG & 8) PAGE();
      const u32 rho = (yi - 108) * 2 + (w & 1);
      const u32 kg = (g & 1) * 2 + (g >> 1);
      u32 tk;
      _Pragma("nounroll") for (u32 sp = 0;; ++sp) {
        asm volatile("" ::: "memory");
        const v4u ta = ld16<16>(rws, a_q, WS_TS), tb = ld16<16>(rws, a_q, WS_TS + 1024);
        int d = dmax<0x140>(dmax<0x141>(dmax<0x4e>(dmax<0xb1>(max((int)(ta.x - tstart), (int)(tb.x - tstart))))));
        tk = tstart + __builtin_amdgcn_readfirstlane(max(max(__builtin_amdgcn_readlane(d, 48), __builtin_amdgcn_readlane(d, 16)), max(__builtin_amdgcn_readlane(d, 32), d)));
        if (!bal((ta.z != tlo) | (ta.w != thi) | (tb.z != tlo) | (tb.w != thi)) || sp >= SPIN_CAP) break;
        __builtin_amdgcn_s_sleep(TS_SLEEP);
      }
      if (w >= 2) {
        v4u yv[7];
        until(tk + DY);
        STAMPB(11);
        _Pragma("nounroll") for (u32 sp = 0;; ++sp) {
          asm volatile("" ::: "memory");
#pragma unroll
          for (int i = 0; i < 7; ++i) yv[i] = ld16<16>(rws, (i == 6 && lane >= 48 ? lane - 48 : lane) * 16 + (i & 3) * 1024, WS_IN5 + rho * 16384 + 8192 + (i >> 2) * 4096);
          bool ok = true;
#pragma unroll
          for (int i = 0; i < 7; ++i) ok &= tagof(yv[i]) == tlo;
          if (__all(ok) || sp >= SPIN_CAP) break;
          __builtin_amdgcn_s_sleep(YP_SLEEP);
        }
        v4f a = lane < 48 ? untag(yv[6]) : (v4f){0.f, 0.f, 0.f, 0.f};
#pragma unroll
        for (int i = 0; i < 6; ++i) a += untag(yv[i]);
        float y = sw16add(sw32add(a.x, a.y), sw32add(a.z, a.w));
        y += dpp<0x128>(y); y += dpp<0x124>(y);
        y = rb(y);
        const u32 n3 = n & 3;
        lds_st<v2u>(L_G + (w & 1) * 128 + (((n3 & 1) * 2 + (n3 >> 1)) * 4 + kg) * 8, (v2u){fu(rb(y / (1.0f + __expf(-y)))), 1u});
        STAMPB(10);
      } else {
        const u32 col = (rho >> 1) * 32 + 8 * kg + 4 * (rho & 1) + (n & 3);
        float xf = bflo(__builtin_amdgcn_raw_buffer_load_b16(rx, col * 2, 0, 0));
        PIN(xf);
        until(tk + DS);
        STAMPB(13);
        v4u sv[8];
        v2u gt;
        _Pragma("nounroll") for (u32 sp = 0;; ++sp) {
          asm volatile("" ::: "memory");
#pragma unroll
          for (int i = 0; i < 8; ++i) sv[i] = ld16<16>(rws, lane * 16 + (i & 3) * 1024, WS_IN5 + rho * 16384 + (i >> 2) * 4096);
          gt = lds_ld<v2u>(L_G + w * 128 + (g * 4 + (n & 3)) * 8);
          bool ok = true;
#pragma unroll
          for (int i = 0; i < 8; ++i) ok &= tagof(sv[i]) == tlo;
          if (__all(ok) || sp >= SPIN_CAP) break;
          if (RP_SLEEP) __builtin_amdgcn_s_sleep(RP_SLEEP);
          CNT(nsp5);
        }
        STAMPB(14);
        v4f a = untag(sv[0]);
#pragma unroll
        for (int i = 1; i < 8; ++i) a += untag(sv[i]);
        float s = sw16add(sw32add(a.x, a.y), sw32add(a.z, a.w));
        s += dpp<0x128>(s); s += dpp<0x124>(s);
        _Pragma("nounroll") for (u32 sp = 0; __builtin_expect(bal(gt.y == 0) != 0, 0) && sp < 64 * SPIN_CAP; ++sp) {
          asm volatile("" ::: "memory");
          gt = lds_ld<v2u>(L_G + w * 128 + (g * 4 + (n & 3)) * 8);
        }
        const u32 o = pk(xf + rb(rb(s) * uf(gt.x)), 0.f);
        if (n < 4) ((unsigned short*)pout)[col] = (unsigned short)o;
        if (rho == 0 && lane == 0) __builtin_amdgcn_raw_buffer_store_b64((v2u){tlo, thi}, rws, 0, 0, 16);
      }
    }
  }
}

extern "C" void kernel_launch(void* const* d_in, const int* in_sizes, int n_in, void* d_out, int out_size, void* d_ws, size_t ws_size,
                              hipStream_t stream) {
  mla_decode_kernel<<<256, 512, 0, stream>>>(d_in[0], d_in[1], d_in[2], d_in[3], d_ws, d_in[4], d_in[5], d_in[7], d_in[8], d_out);
}
```
